```python
import torch

def _softmax(x, axis=-1):
    return torch.softmax(x, dim=axis)
def _gelu(x):
    return torch.nn.functional.gelu(x, approximate="tanh")
def _silu(x):
    return torch.nn.functional.silu(x)
def _sigmoid(x):
    return torch.sigmoid(x)
def _layernorm(x, axis=-1, eps=1e-5):
    m = x.mean(dim=axis, keepdim=True)
    v = x.var(dim=axis, keepdim=True, unbiased=False)
    return (x - m) / torch.sqrt(v + eps)
def _scatter_add(tgt, idx, src, axis=0):
    return tgt.clone().index_add_(axis, idx, src)
def _segment_sum(src, seg, n):
    out = torch.zeros((n,) + src.shape[1:], dtype=src.dtype)
    return out.index_add_(0, seg, src)
def _pad(x, pw):
    flat = tuple(v for pair in reversed(pw) for v in pair)
    return torch.nn.functional.pad(x, flat)

kernel_name = 'gated_mix__sliding_local_dilated__mha_block__prompt'

def setup_inputs():

    _w = (lambda a: (lambda t: t.to(torch.bfloat16) if t.is_floating_point() else t)(torch.as_tensor(a)))
    def _A_setup():
        B, S, D = 1, 2048, 448
        return {
            "x": _w((lambda *s: torch.randn(*s, dtype=torch.bfloat16) * 0.05)(B, S, D)),
            "wqkv": _w((lambda *s: torch.randn(*s, dtype=torch.bfloat16) * 1.6)(D, 3 * D)),
            "wo": _w((lambda *s: torch.randn(*s, dtype=torch.bfloat16) * 0.05)(D, D)),
        }
    _A_in = _A_setup()

    def _B_setup():
        B, S, D, H = 1, 2048, 448, 16
        return {
            "x": _w((lambda *s: torch.randn(*s, dtype=torch.bfloat16) * 0.05)(B, S, D)),
            "wqkv": _w((lambda *s: torch.randn(*s, dtype=torch.bfloat16) * 1.6)(D, 3 * D)),
            "wo": _w((lambda *s: torch.randn(*s, dtype=torch.bfloat16) * 0.1)(D, D)),
        }
    _B_in = _B_setup()

    return {
        "A_x": _A_in["x"],
        "A_wqkv": _A_in["wqkv"],
        "A_wo": _A_in["wo"],
        "B_x": _B_in["x"],
        "B_wqkv": _B_in["wqkv"],
        "B_wo": _B_in["wo"],
        "gate_w": _w((lambda *s: torch.randn(*s, dtype=torch.bfloat16) * 0.05)(448) * 16.0),
    }

def reference(A_x, A_wqkv, A_wo, B_x, B_wqkv, B_wo, gate_w):
    def _A_ref(x, wqkv, wo):
        B, S, D = x.shape
        H = 16; Dh = D // H
        W, dil = 8, 2
        qkv = (lambda x, *d: x.permute(*d))((x @ wqkv).reshape(B, S, 3, H, Dh), 2, 0, 3, 1, 4)
        q, k, v = qkv[0], qkv[1], qkv[2]
        scale = Dh ** -0.5
        pos = torch.arange(S)
        off = torch.arange(W) * dil
        idx = pos[:, None] - off[None, :]
        mask = idx >= 0
        idx_c = torch.where(mask, idx, 0).reshape(-1)
        kg = k[:, :, idx_c, :].reshape(B, H, S, W, Dh)
        vg = v[:, :, idx_c, :].reshape(B, H, S, W, Dh)
        logits = torch.einsum("bhsd,bhswd->bhsw", q, kg) * scale
        logits = torch.where(mask[None, None, :, :], logits, -1e9)
        attn = _softmax(logits, axis=-1)
        o = torch.einsum("bhsw,bhswd->bhsd", attn, vg)
        return (lambda x, *d: x.permute(*d))(o, 0, 2, 1, 3).reshape(B, S, D) @ wo

    def _B_ref(x, wqkv, wo):
        B, S, D = x.shape
        H = 16
        Dh = D // H
        qkv = (lambda x, *d: x.permute(*d))((x @ wqkv).reshape(B, S, 3, H, Dh), 2, 0, 3, 1, 4)
        q, k, v = qkv[0], qkv[1], qkv[2]
        scores = torch.einsum("bhqd,bhkd->bhqk", q, k) * (Dh ** -0.5)
        p = _softmax(scores, axis=-1)
        o = torch.einsum("bhqk,bhkd->bhqd", p, v)
        return (lambda x, *d: x.permute(*d))(o, 0, 2, 1, 3).reshape(B, S, D) @ wo

    x = A_x
    g = _sigmoid((x * gate_w).sum(-1, keepdims=True))
    a = _A_ref(x, A_wqkv, A_wo)
    b = _B_ref(x, B_wqkv, B_wo)
    return g * a + (1 - g) * b
```

```cpp
#include <hip/hip_runtime.h>
#include <hip/hip_bf16.h>
#include <stdint.h>

typedef unsigned short u16;
typedef short bf16x8 __attribute__((ext_vector_type(8)));
typedef short s16x4 __attribute__((ext_vector_type(4)));
typedef float f32x16 __attribute__((ext_vector_type(16)));
typedef float f32x2 __attribute__((ext_vector_type(2)));
typedef float f32x4 __attribute__((ext_vector_type(4)));
typedef unsigned u32x4 __attribute__((ext_vector_type(4)));
typedef unsigned u32x2 __attribute__((ext_vector_type(2)));
typedef __bf16 bf2_t __attribute__((ext_vector_type(2)));
typedef __attribute__((address_space(3))) const char* lds_cptr;
typedef __attribute__((address_space(3))) char* lds_ptr;
typedef short v4i16_t __attribute__((ext_vector_type(4)));

#define DI __device__ __forceinline__
#define MFMA(a, b, c) __builtin_amdgcn_mfma_f32_32x32x16_bf16((a), (b), (c), 0, 0, 0)

constexpr int S_ = 2048, D_ = 448, H_ = 16, DH = 28;
constexpr float QSCALE = 0.18898223650461362f * 1.4426950408889634f;

constexpr size_t WS_QF = 0;
constexpr size_t WS_KV = WS_QF + (size_t)4 * 1048576;
constexpr size_t WS_OF = WS_KV + (size_t)8 * 1048576;

DI unsigned pk2(float a, float b) {
  f32x2 v = {a, b};
  bf2_t r = __builtin_convertvector(v, bf2_t);
  return __builtin_bit_cast(unsigned, r);
}
DI bf16x8 pack8(const f32x16& x, int o) {
  u32x4 p;
  p[0] = pk2(x[o + 0], x[o + 1]); p[1] = pk2(x[o + 2], x[o + 3]);
  p[2] = pk2(x[o + 4], x[o + 5]); p[3] = pk2(x[o + 6], x[o + 7]);
  return __builtin_bit_cast(bf16x8, p);
}
DI void glds16(const void* g, unsigned lds_base) {
  unsigned sv;
  asm volatile("s_mov_b32 %0, m0\n\ts_mov_b32 m0, %2\n\ts_nop 0\n\tglobal_load_lds_dwordx4 %1, off\n\ts_mov_b32 m0, %0"
               : "=&s"(sv) : "v"(g), "s"(lds_base) : "memory");
}
DI s16x4 trrd(lds_cptr p) {
  return __builtin_bit_cast(s16x4, __builtin_amdgcn_ds_read_tr16_b64_v4i16((__attribute__((address_space(3))) v4i16_t*)p));
}
DI bf16x8 cat8(s16x4 lo, s16x4 hi) { return __builtin_shufflevector(lo, hi, 0, 1, 2, 3, 4, 5, 6, 7); }
DI bf16x8 ldsrd16(lds_cptr p) { return *(const __attribute__((address_space(3))) bf16x8*)p; }
DI float xhalf_max(float m) {
  auto rr = __builtin_amdgcn_permlane32_swap(__float_as_uint(m), __float_as_uint(m), false, false);
  return fmaxf(__uint_as_float(rr[0]), __uint_as_float(rr[1]));
}
DI float max16(const f32x16& s) {
  float a = fmaxf(fmaxf(s[0], s[1]), s[2]), b = fmaxf(fmaxf(s[3], s[4]), s[5]);
  a = fmaxf(fmaxf(a, s[6]), s[7]); b = fmaxf(fmaxf(b, s[8]), s[9]);
  a = fmaxf(fmaxf(a, s[10]), s[11]); b = fmaxf(fmaxf(b, s[12]), s[13]);
  a = fmaxf(fmaxf(a, s[14]), s[15]);
  return fmaxf(a, b);
}

constexpr int K1_SLOT = 40960;
constexpr int K1_LDS = 3 * K1_SLOT + 64;

__global__ __launch_bounds__(512, 1) void k1_qkv(const u16* __restrict__ x, const u16* __restrict__ wA,
                                                 const u16* __restrict__ wB, char* __restrict__ ws) {
  extern __shared__ __attribute__((aligned(16))) char lds[];
  const int tid = threadIdx.x, lane = tid & 63;
  const int wid = __builtin_amdgcn_readfirstlane(tid >> 6);
  const int b = blockIdx.x, h = (b & 7) + 8 * ((b >> 3) & 1), rb = b >> 4;
  const int r32 = lane & 31, hh = lane >> 5;
  const unsigned lds0 = (unsigned)(uintptr_t)lds;
  const int win = (56 * h) & ~15, woff = (56 * h) & 15;

  const char* xs[2];
#pragma unroll
  for (int i = 0; i < 2; ++i) {
    const int p = wid + 8 * i, row = 8 * p + (lane >> 3), c = (lane & 7) ^ ((row >> 1) & 7);
    xs[i] = (const char*)x + (size_t)(rb * 128 + row) * 896 + c * 16;
  }
  const char* wsrc[3];
#pragma unroll
  for (int i = 0; i < 3; ++i) {
    const int q = wid + 8 * i, sl = q >> 2, pp = q & 3;
    const char* base = (const char*)(sl < 3 ? wB : wA);
    const int sec = sl < 3 ? sl : sl - 3;
    wsrc[i] = base + (size_t)(16 * pp + (lane >> 2)) * 2688 + sec * 896 + win + (lane & 3) * 16;
  }
#define K1_ISSUE(c, slot)                                                                         \
  do {                                                                                            \
    const unsigned sb = lds0 + (slot) * K1_SLOT;                                                  \
    glds16(xs[0] + (c) * 128, sb + wid * 1024);                                                   \
    glds16(xs[1] + (c) * 128, sb + (wid + 8) * 1024);                                             \
    glds16(wsrc[0] + (size_t)(c) * 64 * 2688, sb + 16384 + wid * 1024);                           \
    glds16(wsrc[1] + (size_t)(c) * 64 * 2688, sb + 16384 + (wid + 8) * 1024);                     \
    glds16(wsrc[2] + (size_t)(c) * 64 * 2688, sb + 16384 + (wid + 16) * 1024);                    \
  } while (0)

  const int rt = wid & 3, cg = wid >> 2;
  const int xrow = rt * 32 + r32;
  const int xsw = (xrow >> 1) & 7;
  const int g16 = lane >> 4, i16 = lane & 15, q4 = i16 >> 2, p4 = i16 & 3, cb = g16 & 1;
  const int wbase = 16384 + cg * 3 * 4096 + woff + (8 * hh + q4) * 64 + cb * 32 + p4 * 8;

  f32x16 acc0 = {}, acc1 = {}, acc2 = {};
  K1_ISSUE(0, 0);
  K1_ISSUE(1, 1);
  int slot = 0;
  for (int c = 0; c < 7; ++c) {
    if (c < 6) asm volatile("s_waitcnt vmcnt(5) lgkmcnt(0)\n\ts_barrier" ::: "memory");
    else asm volatile("s_waitcnt vmcnt(0) lgkmcnt(0)\n\ts_barrier" ::: "memory");
    if (c + 2 < 7) {
      const int ns = slot == 0 ? 2 : slot - 1;
      K1_ISSUE(c + 2, ns);
    }
    lds_cptr sb = (lds_cptr)lds + slot * K1_SLOT;
#pragma unroll
    for (int s = 0; s < 4; ++s) {
      const bf16x8 xf = ldsrd16(sb + xrow * 128 + (((2 * s + hh) ^ xsw) * 16));
      lds_cptr wp = sb + wbase + s * 1024;
      const bf16x8 w0 = cat8(trrd(wp), trrd(wp + 256));
      const bf16x8 w1 = cat8(trrd(wp + 4096), trrd(wp + 4096 + 256));
      const bf16x8 w2 = cat8(trrd(wp + 8192), trrd(wp + 8192 + 256));
      acc0 = MFMA(w0, xf, acc0);
      acc1 = MFMA(w1, xf, acc1);
      acc2 = MFMA(xf, w2, acc2);
    }
    slot = slot == 2 ? 0 : slot + 1;
  }
#undef K1_ISSUE
  const int br = cg;
#pragma unroll
  for (int r = 0; r < 16; ++r) acc0[r] *= QSCALE;
  if (hh) {
    acc0[12] = 0.f; acc0[13] = 0.f; acc0[14] = 0.f; acc0[15] = 0.f;
    acc1[12] = 1.f; acc1[13] = 0.f; acc1[14] = 0.f; acc1[15] = 0.f;
  }
  if (r32 >= 28) {
    const float f = r32 == 28 ? 1.f : 0.f;
#pragma unroll
    for (int r = 0; r < 16; ++r) acc2[r] = f;
  }
  const int t64 = rb * 4 + rt;
  char* qd = ws + WS_QF + ((size_t)((br * 16 + h) * 64 + t64)) * 2048 + lane * 16;
  *(bf16x8*)qd = pack8(acc0, 0);
  *(bf16x8*)(qd + 1024) = pack8(acc0, 8);
  char* kd = ws + WS_KV + ((size_t)((br * 16 + h) * 64 + t64)) * 4096 + lane * 16;
  *(bf16x8*)kd = pack8(acc1, 0);
  *(bf16x8*)(kd + 1024) = pack8(acc1, 8);
  *(bf16x8*)(kd + 2048) = pack8(acc2, 0);
  *(bf16x8*)(kd + 3072) = pack8(acc2, 8);
}

constexpr int K2_LDS = 65536 + 2048;

__global__ __launch_bounds__(512, 1) void k2_attn(char* __restrict__ ws) {
  extern __shared__ __attribute__((aligned(16))) char lds[];
  const int tid = threadIdx.x, lane = tid & 63;
  const int wid = __builtin_amdgcn_readfirstlane(tid >> 6);
  const int b = blockIdx.x, h = (b & 7) + 8 * ((b >> 3) & 1), rb = b >> 4;
  const int r32 = lane & 31, hh = lane >> 5;
  const f32x16 zero = {};

  if (wid < 4) {
    const int t64 = rb * 4 + wid;
    const char* qp = ws + WS_QF + ((size_t)((16 + h) * 64 + t64)) * 2048 + lane * 16;
    const char* kc = ws + WS_KV + ((size_t)((16 + h) * 64 + t64)) * 4096 + lane * 16;
    const char* kp = t64 > 0 ? kc - 4096 : kc;
    const bf16x8 q0 = *(const bf16x8*)qp, q1 = *(const bf16x8*)(qp + 1024);
    const bf16x8 kc0 = *(const bf16x8*)kc, kc1 = *(const bf16x8*)(kc + 1024);
    const bf16x8 vc0 = *(const bf16x8*)(kc + 2048), vc1 = *(const bf16x8*)(kc + 3072);
    const bf16x8 kp0 = *(const bf16x8*)kp, kp1 = *(const bf16x8*)(kp + 1024);
    const bf16x8 vp0 = *(const bf16x8*)(kp + 2048), vp1 = *(const bf16x8*)(kp + 3072);
    f32x16 sc = MFMA(kc0, q0, zero); sc = MFMA(kc1, q1, sc);
    f32x16 sp = MFMA(kp0, q0, zero); sp = MFMA(kp1, q1, sp);
#pragma unroll
    for (int r = 0; r < 16; ++r) {
      const int i = (r & 3) + 8 * (r >> 2) + 4 * hh;
      const int dc = r32 - i, dp = r32 + 32 - i;
      const bool vc = (dc >= 0) && (dc <= 14) && !(dc & 1);
      const bool vp = (dp <= 14) && !(dp & 1) && (t64 > 0);
      sc[r] = vc ? sc[r] : -INFINITY;
      sp[r] = vp ? sp[r] : -INFINITY;
    }
    const float m = xhalf_max(fmaxf(max16(sc), max16(sp)));
#pragma unroll
    for (int r = 0; r < 16; ++r) {
      sc[r] = __builtin_amdgcn_exp2f(sc[r] - m);
      sp[r] = __builtin_amdgcn_exp2f(sp[r] - m);
    }
    f32x16 o = MFMA(vc0, pack8(sc, 0), zero);
    o = MFMA(vc1, pack8(sc, 8), o);
    o = MFMA(vp0, pack8(sp, 0), o);
    o = MFMA(vp1, pack8(sp, 8), o);
    float l = o[12];
    l = __shfl(l, 32 + r32);
    const float rl = 1.0f / l;
#pragma unroll
    for (int r = 0; r < 16; ++r) o[r] *= rl;
    if (hh) { o[12] = 0.f; o[13] = 0.f; o[14] = 0.f; o[15] = 0.f; }
    char* od = ws + WS_OF + ((size_t)((64 + t64) * 16 + h)) * 2048 + lane * 16;
    *(bf16x8*)od = pack8(o, 0);
    *(bf16x8*)(od + 1024) = pack8(o, 8);
  }

  const int qg = wid & 1, ks = wid >> 1;
  const char* qp = ws + WS_QF + ((size_t)(h * 64 + rb * 4 + 2 * qg)) * 2048 + lane * 16;
  bf16x8 qa0 = *(const bf16x8*)qp, qa1 = *(const bf16x8*)(qp + 1024);
  bf16x8 qb0 = *(const bf16x8*)(qp + 2048), qb1 = *(const bf16x8*)(qp + 3072);
  const char* kvh = ws + WS_KV + (size_t)(h * 64) * 4096 + lane * 16;
  {
    const bf16x8 k0 = *(const bf16x8*)kvh, k1 = *(const bf16x8*)(kvh + 1024);
    f32x16 sa = MFMA(k0, qa0, zero); sa = MFMA(k1, qa1, sa);
    f32x16 sb = MFMA(k0, qb0, zero); sb = MFMA(k1, qb1, sb);
    const float ma = xhalf_max(max16(sa)), mb = xhalf_max(max16(sb));
    const unsigned na = pk2(-ma, 0.f), nb = pk2(-mb, 0.f);
    u32x4 ta = __builtin_bit_cast(u32x4, qa1), tb = __builtin_bit_cast(u32x4, qb1);
    if (hh) { ta[2] = na; tb[2] = nb; }
    qa1 = __builtin_bit_cast(bf16x8, ta); qb1 = __builtin_bit_cast(bf16x8, tb);
  }
  const char* kv = kvh + (size_t)(ks * 16) * 4096;
  f32x16 oa = zero, ob = zero;
  bf16x8 k0 = *(const bf16x8*)kv, k1 = *(const bf16x8*)(kv + 1024);
  bf16x8 v0 = *(const bf16x8*)(kv + 2048), v1 = *(const bf16x8*)(kv + 3072);
  for (int t = 0; t < 16; ++t) {
    const char* nx = kv + (size_t)(t < 15 ? t + 1 : t) * 4096;
    const bf16x8 nk0 = *(const bf16x8*)nx, nk1 = *(const bf16x8*)(nx + 1024);
    const bf16x8 nv0 = *(const bf16x8*)(nx + 2048), nv1 = *(const bf16x8*)(nx + 3072);
    f32x16 sa = MFMA(k0, qa0, zero); sa = MFMA(k1, qa1, sa);
    f32x16 sb = MFMA(k0, qb0, zero); sb = MFMA(k1, qb1, sb);
#pragma unroll
    for (int r = 0; r < 16; ++r) sa[r] = __builtin_amdgcn_exp2f(sa[r]);
    oa = MFMA(v0, pack8(sa, 0), oa);
    oa = MFMA(v1, pack8(sa, 8), oa);
#pragma unroll
    for (int r = 0; r < 16; ++r) sb[r] = __builtin_amdgcn_exp2f(sb[r]);
    ob = MFMA(v0, pack8(sb, 0), ob);
    ob = MFMA(v1, pack8(sb, 8), ob);
    k0 = nk0; k1 = nk1; v0 = nv0; v1 = nv1;
  }
  lds_ptr cb = (lds_ptr)lds + qg * 32768 + lane * 16;
  {
    typedef __attribute__((address_space(3))) f32x4* lf4;
#define WRQ(dst, O, o8)                                                                           \
  if ((dst) != ks) {                                                                              \
    lds_ptr p = cb + ((dst) * 4 + ks) * 2048;                                                     \
    *(lf4)p = (f32x4){O[o8], O[o8 + 1], O[o8 + 2], O[o8 + 3]};                                    \
    *(lf4)(p + 1024) = (f32x4){O[o8 + 4], O[o8 + 5], O[o8 + 6], O[o8 + 7]};                       \
  }
    WRQ(0, oa, 0) WRQ(1, oa, 8) WRQ(2, ob, 0) WRQ(3, ob, 8)
#undef WRQ
    __attribute__((address_space(3))) float* lb = (__attribute__((address_space(3))) float*)((lds_ptr)lds + 65536) + qg * 256;
    if (hh) { lb[ks * 32 + r32] = oa[12]; lb[128 + ks * 32 + r32] = ob[12]; }
  }
  __syncthreads();
  {
    typedef __attribute__((address_space(3))) const f32x4* lf4c;
    float own[8];
    if (ks == 0) { for (int i = 0; i < 8; ++i) own[i] = oa[i]; }
    else if (ks == 1) { for (int i = 0; i < 8; ++i) own[i] = oa[8 + i]; }
    else if (ks == 2) { for (int i = 0; i < 8; ++i) own[i] = ob[i]; }
    else { for (int i = 0; i < 8; ++i) own[i] = ob[8 + i]; }
    float sum[8];
#pragma unroll
    for (int i = 0; i < 8; ++i) sum[i] = 0.f;
#pragma unroll
    for (int src = 0; src < 4; ++src) {
      if (src == ks) {
#pragma unroll
        for (int i = 0; i < 8; ++i) sum[i] += own[i];
      } else {
        lds_cptr p = (lds_cptr)cb + (ks * 4 + src) * 2048;
        const f32x4 u = *(lf4c)p, w = *(lf4c)(p + 1024);
        sum[0] += u[0]; sum[1] += u[1]; sum[2] += u[2]; sum[3] += u[3];
        sum[4] += w[0]; sum[5] += w[1]; sum[6] += w[2]; sum[7] += w[3];
      }
    }
    const __attribute__((address_space(3))) float* lb =
        (const __attribute__((address_space(3))) float*)((lds_cptr)lds + 65536) + qg * 256 + (ks >> 1) * 128 + r32;
    const float l = (lb[0] + lb[32]) + (lb[64] + lb[96]);
    const float rl = 1.0f / l;
#pragma unroll
    for (int i = 0; i < 8; ++i) sum[i] *= rl;
    if (hh && (ks & 1)) { sum[4] = 0.f; sum[5] = 0.f; sum[6] = 0.f; sum[7] = 0.f; }
    u32x4 pkd = {pk2(sum[0], sum[1]), pk2(sum[2], sum[3]), pk2(sum[4], sum[5]), pk2(sum[6], sum[7])};
    const int t64 = rb * 4 + 2 * qg + (ks >> 1);
    char* od = ws + WS_OF + ((size_t)(t64 * 16 + h)) * 2048 + (ks & 1) * 1024 + lane * 16;
    *(u32x4*)od = pkd;
  }
}

constexpr int K3_WO = 452 * 112;
constexpr int K3_ACC = 2 * K3_WO + 64;
constexpr int K3_G = K3_ACC + 4 * 4096;
constexpr int K3_LDS = K3_G + 256;

__global__ __launch_bounds__(512, 1) void k3_out(const u16* __restrict__ x, const u16* __restrict__ woA,
                                                 const u16* __restrict__ woB, const u16* __restrict__ gw,
                                                 const char* __restrict__ ws, u16* __restrict__ out) {
  extern __shared__ __attribute__((aligned(16))) char lds[];
  const int tid = threadIdx.x, lane = tid & 63;
  const int wid = __builtin_amdgcn_readfirstlane(tid >> 6);
  const int j = blockIdx.x, r0 = (j >> 3) * 64, c0 = (j & 7) * 56;
  const int r32 = lane & 31, hh = lane >> 5;
  const unsigned lds0 = (unsigned)(uintptr_t)lds;
  for (int p = wid; p < 98; p += 8) {
    const int br = p >= 49, pp = br ? p - 49 : p;
    const int q = pp * 64 + lane, row = q / 7, c = q - row * 7;
    const char* src = (const char*)(br ? woA : woB) + (size_t)row * 896 + c0 * 2 + c * 16;
    glds16(src, lds0 + br * K3_WO + pp * 1024);
  }
  if (tid < 64) {
    typedef __attribute__((address_space(3))) u32x2* lu2;
    if (tid < 56) {
      *(lu2)((lds_ptr)lds + 448 * 112 + tid * 8) = (u32x2){0u, 0u};
      *(lu2)((lds_ptr)lds + K3_WO + 448 * 112 + tid * 8) = (u32x2){0u, 0u};
    } else {
      *(lu2)((lds_ptr)lds + 2 * K3_WO + (tid - 56) * 8) = (u32x2){0u, 0u};
    }
  }
  const int rt = wid & 1, ct = (wid >> 1) & 1, br = wid >> 2;
  const char* op = ws + WS_OF + ((size_t)((br * 64 + (r0 >> 5) + rt) * 16)) * 2048 + lane * 16;
  bf16x8 of[32];
#pragma unroll
  for (int i = 0; i < 32; ++i) of[i] = *(const bf16x8*)(op + i * 1024);
  {
    __attribute__((address_space(3))) float* gb = (__attribute__((address_space(3))) float*)((lds_ptr)lds + K3_G);
    float gwf[8];
    if (lane < 56) {
      const bf16x8 g8 = *(const bf16x8*)(gw + lane * 8);
#pragma unroll
      for (int i = 0; i < 8; ++i) gwf[i] = __uint_as_float(((unsigned)(u16)g8[i]) << 16);
    } else {
#pragma unroll
      for (int i = 0; i < 8; ++i) gwf[i] = 0.f;
    }
#pragma unroll
    for (int i = 0; i < 8; ++i) {
      const int row = r0 + wid * 8 + i;
      float sacc = 0.f;
      if (lane < 56) {
        const bf16x8 x8 = *(const bf16x8*)(x + (size_t)row * 448 + lane * 8);
#pragma unroll
        for (int e = 0; e < 8; ++e) sacc += __uint_as_float(((unsigned)(u16)x8[e]) << 16) * gwf[e];
      }
#pragma unroll
      for (int o = 32; o >= 1; o >>= 1) sacc += __shfl_xor(sacc, o);
      if (lane == 0) gb[wid * 8 + i] = 1.0f / (1.0f + __expf(-sacc));
    }
  }
  asm volatile("s_waitcnt vmcnt(0) lgkmcnt(0)\n\ts_barrier" ::: "memory");
  const int g16 = lane >> 4, i16 = lane & 15, q4 = i16 >> 2, p4 = i16 & 3, cb = g16 & 1;
  lds_cptr wp = (lds_cptr)lds + br * K3_WO + (4 * hh + q4) * 112 + (ct * 32 + cb * 16 + 4 * p4) * 2;
  f32x16 acc = {};
#pragma unroll
  for (int hd = 0; hd < 16; ++hd) {
#pragma unroll
    for (int s = 0; s < 2; ++s) {
      lds_cptr p = wp + (hd * 28 + 16 * s) * 112;
      const bf16x8 wf = cat8(trrd(p), trrd(p + 8 * 112));
      acc = MFMA(wf, of[hd * 2 + s], acc);
    }
  }
  typedef __attribute__((address_space(3))) f32x4* lf4;
  lds_ptr ab = (lds_ptr)lds + K3_ACC + (wid & 3) * 4096 + lane * 16;
  if (br == 1) {
#pragma unroll
    for (int g = 0; g < 4; ++g) *(lf4)(ab + g * 1024) = (f32x4){acc[4 * g], acc[4 * g + 1], acc[4 * g + 2], acc[4 * g + 3]};
  }
  __syncthreads();
  if (br == 0) {
    const float g = ((const __attribute__((address_space(3))) float*)((lds_cptr)lds + K3_G))[rt * 32 + r32];
    const int row = r0 + rt * 32 + r32;
#pragma unroll
    for (int gq = 0; gq < 4; ++gq) {
      const f32x4 a = *(lf4)(ab + gq * 1024);
      const int col = ct * 32 + 8 * gq + 4 * hh;
      if (col < 56) {
        float o0 = g * a[0] + (1.f - g) * acc[4 * gq], o1 = g * a[1] + (1.f - g) * acc[4 * gq + 1];
        float o2 = g * a[2] + (1.f - g) * acc[4 * gq + 2], o3 = g * a[3] + (1.f - g) * acc[4 * gq + 3];
        u32x2 w = {pk2(o0, o1), pk2(o2, o3)};
        *(u32x2*)(out + (size_t)row * 448 + c0 + col) = w;
      }
    }
  }
}

extern "C" void kernel_launch(void* const* d_in, const int* in_sizes, int n_in, void* d_out, int out_size,
                              void* d_ws, size_t ws_size, hipStream_t stream) {
  const u16* x = (const u16*)d_in[0];
  const u16* wqkvA = (const u16*)d_in[1];
  const u16* woA = (const u16*)d_in[2];
  const u16* wqkvB = (const u16*)d_in[4];
  const u16* woB = (const u16*)d_in[5];
  const u16* gw = (const u16*)d_in[6];
  char* ws = (char*)d_ws;
  (void)hipFuncSetAttribute((const void*)k1_qkv, hipFuncAttributeMaxDynamicSharedMemorySize, K1_LDS);
  (void)hipFuncSetAttribute((const void*)k2_attn, hipFuncAttributeMaxDynamicSharedMemorySize, K2_LDS);
  (void)hipFuncSetAttribute((const void*)k3_out, hipFuncAttributeMaxDynamicSharedMemorySize, K3_LDS);
  hipLaunchKernelGGL(k1_qkv, dim3(256), dim3(512), K1_LDS, stream, x, wqkvA, wqkvB, ws);
  hipLaunchKernelGGL(k2_attn, dim3(256), dim3(512), K2_LDS, stream, ws);
  hipLaunchKernelGGL(k3_out, dim3(256), dim3(512), K3_LDS, stream, x, woA, woB, gw, (const char*)ws, (u16*)d_out);
}
```

```cpp
#include <hip/hip_runtime.h>
#include <hip/hip_bf16.h>
#include <stdint.h>

typedef unsigned short u16;
typedef unsigned long long u64;
typedef short bf16x8 __attribute__((ext_vector_type(8)));
typedef short s16x4 __attribute__((ext_vector_type(4)));
typedef float f32x16 __attribute__((ext_vector_type(16)));
typedef float f32x2 __attribute__((ext_vector_type(2)));
typedef float f32x4 __attribute__((ext_vector_type(4)));
typedef unsigned u32x4 __attribute__((ext_vector_type(4)));
typedef unsigned u32x2 __attribute__((ext_vector_type(2)));
typedef __bf16 bf2_t __attribute__((ext_vector_type(2)));
typedef __attribute__((address_space(3))) const char* lds_cptr;
typedef __attribute__((address_space(3))) char* lds_ptr;
typedef __attribute__((address_space(1))) u64 gu64;
typedef short v4i16_t __attribute__((ext_vector_type(4)));

#define DI __device__ __forceinline__
#define POLL_SLEEP 10
#define STAMP(i)
#define STAMPW(i, w)
#define STAMPDUMP()
#define MFMA(a, b, c) __builtin_amdgcn_mfma_f32_32x32x16_bf16((a), (b), (c), 0, 0, 0)
#define BARRIER() asm volatile("s_waitcnt lgkmcnt(0)\n\ts_barrier" ::: "memory")
#define WAITBAR(N) asm volatile("s_waitcnt vmcnt(" #N ") lgkmcnt(0)\n\ts_barrier" ::: "memory")

extern "C" __device__ u64 gm_dispatch_id() __asm("llvm.amdgcn.dispatch.id");

constexpr float QSCALE = 0.18898223650461362f * 1.4426950408889634f;

constexpr unsigned WS_QF = 0;
constexpr unsigned WS_KV = 4u << 20;
constexpr unsigned WS_OF = 12u << 20;
constexpr unsigned WS_G = 16u << 20;
constexpr unsigned WS_FA = WS_G + 65536;
constexpr unsigned WS_FB = WS_FA + 32768;
constexpr unsigned WS_F3 = WS_FB + 32768;
constexpr unsigned WS_F3A = WS_F3 + 32768;
constexpr unsigned WS_END = WS_F3A + 131072;

constexpr int LDS_BYTES = 163840;
constexpr int L_X = 0;
constexpr int L_WR = 98304;
constexpr int L_GW = 147456;
constexpr int L_CNT = 148480;
constexpr int L_CB = 0;
constexpr int L_LB = 131072;
constexpr int L_WO = 0;
constexpr int WO_BR = 452 * 128;

DI unsigned pk2(float a, float b) {
  f32x2 v = {a, b};
  bf2_t r = __builtin_convertvector(v, bf2_t);
  return __builtin_bit_cast(unsigned, r);
}
DI u32x4 pack8u(const f32x16& x, int o) {
  u32x4 p;
  p[0] = pk2(x[o + 0], x[o + 1]); p[1] = pk2(x[o + 2], x[o + 3]);
  p[2] = pk2(x[o + 4], x[o + 5]); p[3] = pk2(x[o + 6], x[o + 7]);
  return p;
}
DI bf16x8 pack8(const f32x16& x, int o) { return __builtin_bit_cast(bf16x8, pack8u(x, o)); }
DI void glds16(const void* sbase, unsigned voff, unsigned lds_base) {
  unsigned sv;
  asm volatile("s_mov_b32 %0, m0\n\ts_mov_b32 m0, %3\n\ts_nop 4\n\tglobal_load_lds_dwordx4 %1, %2\n\ts_mov_b32 m0, %0"
               : "=&s"(sv) : "v"(voff), "s"(sbase), "s"(lds_base) : "memory");
}
DI s16x4 trrd(lds_cptr p) {
  return __builtin_bit_cast(s16x4, __builtin_amdgcn_ds_read_tr16_b64_v4i16((__attribute__((address_space(3))) v4i16_t*)p));
}
DI bf16x8 cat8(s16x4 lo, s16x4 hi) { return __builtin_shufflevector(lo, hi, 0, 1, 2, 3, 4, 5, 6, 7); }
DI bf16x8 ldsrd16(lds_cptr p) { return *(const __attribute__((address_space(3))) bf16x8*)p; }
DI float xhalf_max(float m) {
  auto rr = __builtin_amdgcn_permlane32_swap(__float_as_uint(m), __float_as_uint(m), false, false);
  return fmaxf(__uint_as_float(rr[0]), __uint_as_float(rr[1]));
}
DI float xhalf_sum(float m) {
  auto rr = __builtin_amdgcn_permlane32_swap(__float_as_uint(m), __float_as_uint(m), false, false);
  return __uint_as_float(rr[0]) + __uint_as_float(rr[1]);
}
DI float max16(const f32x16& s) {
  float a = fmaxf(fmaxf(s[0], s[1]), s[2]), b = fmaxf(fmaxf(s[3], s[4]), s[5]);
  a = fmaxf(fmaxf(a, s[6]), s[7]); b = fmaxf(fmaxf(b, s[8]), s[9]);
  a = fmaxf(fmaxf(a, s[10]), s[11]); b = fmaxf(fmaxf(b, s[12]), s[13]);
  a = fmaxf(fmaxf(a, s[14]), s[15]);
  return fmaxf(a, b);
}
DI bf16x8 ldg(__amdgpu_buffer_rsrc_t rs, unsigned voff, unsigned soff) {
  return __builtin_bit_cast(bf16x8, __builtin_amdgcn_raw_buffer_load_b128(rs, voff, soff, 16));
}
DI void stg(__amdgpu_buffer_rsrc_t rs, u32x4 v, unsigned voff, unsigned soff) {
  __builtin_amdgcn_raw_buffer_store_b128(v, rs, voff, soff, 16);
}
DI void wait_flags(const char* ws, unsigned off, bool active, u64 tag) {
  gu64* p = (gu64*)(ws + off);
  const u64 t0 = __builtin_amdgcn_s_memrealtime();
  for (;;) {
    u64 v = tag;
    if (active) v = __hip_atomic_load(p, __ATOMIC_RELAXED, __HIP_MEMORY_SCOPE_AGENT);
    if (__all(v == tag)) break;
    __builtin_amdgcn_s_sleep(1);
    if (__builtin_amdgcn_s_memrealtime() - t0 > 5000000ull) break;
  }
  __builtin_amdgcn_fence(__ATOMIC_ACQUIRE, "wavefront");
}
DI void set_flag(char* ws, unsigned off, u64 tag) {
  __hip_atomic_store((gu64*)(ws + off), tag, __ATOMIC_RELAXED, __HIP_MEMORY_SCOPE_AGENT);
}

DI void poll_fa(const char* ws, int lane, int h, int rb, u64 tag) {
  const int cw0 = (28 * h) >> 6, cw1 = (28 * h + 27) >> 6;
  const int q = lane >> 4, cw = (q & 1) ? cw1 : cw0;
  const int g = q < 2 ? (7 + cw) >> 1 : 7 + cw;
  gu64* p0 = (gu64*)(ws + WS_FA + (g * 16 + (lane & 15)) * 128);
  gu64* p1 = (gu64*)(ws + WS_FA + ((((lane & 1) ? cw1 : cw0) >> 1) * 16 + rb) * 128);
  const u64 t0 = __builtin_amdgcn_s_memrealtime();
  for (;;) {
    const u64 v0 = __hip_atomic_load(p0, __ATOMIC_RELAXED, __HIP_MEMORY_SCOPE_AGENT);
    u64 v1 = tag;
    if (lane < 2) v1 = __hip_atomic_load(p1, __ATOMIC_RELAXED, __HIP_MEMORY_SCOPE_AGENT);
    if (__all(v0 == tag && v1 == tag)) break;
    __builtin_amdgcn_s_sleep(1);
    if (__builtin_amdgcn_s_memrealtime() - t0 > 5000000ull) break;
  }
  __builtin_amdgcn_fence(__ATOMIC_ACQUIRE, "wavefront");
}

struct P1Ctx {
  const char* x; const char* w1; const char* w2; const char* w3;
  unsigned lds0, xoff0, xoff1, woff;
  int wid, lane, rot;
};
DI int rotc(const P1Ctx& c, int ch) { const int t = ch + c.rot; return t >= 7 ? t - 7 : t; }
DI void p1_x(const P1Ctx& c, int ch) {
  const int slot = ch == 6 ? 0 : ch;
  const char* src = c.x + rotc(c, ch) * 128;
  glds16(src, c.xoff0, c.lds0 + L_X + slot * 16384 + c.wid * 1024);
  glds16(src, c.xoff1, c.lds0 + L_X + slot * 16384 + (c.wid + 8) * 1024);
}
DI void p1_w12(const P1Ctx& c, int ch) {
  const int slot = ch % 3;
  const int ro = rotc(c, ch) * (64 * 2688);
  glds16(c.w1 + ro, c.woff, c.lds0 + L_WR + slot * 16384 + c.wid * 1024);
  glds16(c.w2 + ro, c.woff, c.lds0 + L_WR + slot * 16384 + 8192 + c.wid * 1024);
}
DI constexpr int w3_off(int ch) { return L_X + (1 + (ch >> 1)) * 16384 + (ch & 1) * 8192; }
DI void p1_w3(const P1Ctx& c, int ch) {
  glds16(c.w3 + rotc(c, ch) * (64 * 2688), c.woff, c.lds0 + w3_off(ch) + c.wid * 1024);
}

template <bool ISV>
DI void p1_store(const f32x16& a, int br, int w, int ct, int t64, int lane, __amdgpu_buffer_rsrc_t rs) {
  const int r32 = lane & 31, hh = lane >> 5;
  const int sec = w / 7, wc = (w - sec * 7) * 64;
  if (ISV) {
    const int c = wc + ct * 32 + r32, head = c / 28, d = c - head * 28;
    const unsigned off = WS_KV + ((br * 16 + head) * 64 + t64) * 4096 + 2048 + (hh * 32 + d) * 16;
    stg(rs, pack8u(a, 0), off, 0);
    stg(rs, pack8u(a, 8), off + 1024, 0);
    if (d >= 24) {
      const unsigned f = d == 24 ? 0x3F803F80u : 0u;
      const u32x4 pv = {f, f, f, f};
      stg(rs, pv, off + 64, 0);
      stg(rs, pv, off + 64 + 1024, 0);
    }
  } else {
    const float sc = sec == 0 ? QSCALE : 1.f;
    unsigned w8[8];
#pragma unroll
    for (int i = 0; i < 8; ++i) w8[i] = pk2(a[2 * i] * sc, a[2 * i + 1] * sc);
    const unsigned padv = sec == 0 ? 0u : 0x3F80u;
    const unsigned hstride = sec == 0 ? 64u * 2048u : 64u * 4096u;
    const unsigned base0 = (sec == 0 ? WS_QF + (br * 16 * 64 + t64) * 2048 : WS_KV + (br * 16 * 64 + t64) * 4096) + r32 * 16;
#pragma unroll
    for (int pp = 0; pp < 2; ++pp) {
      const auto s0 = __builtin_amdgcn_permlane32_swap(w8[4 * pp], w8[4 * pp + 2], false, false);
      const auto s1 = __builtin_amdgcn_permlane32_swap(w8[4 * pp + 1], w8[4 * pp + 3], false, false);
      const int C = (wc >> 3) + ct * 4 + 2 * pp + hh, hp = C / 7, r = C - hp * 7;
      const int head = 2 * hp + (r > 3 ? 1 : 0), cc = r > 3 ? r - 3 : r;
      u32x4 v = {s0[0], s1[0], s0[1], s1[1]};
      if (r == 3) {
        const u32x4 v2 = {padv, 0u, s0[1], s1[1]};
        stg(rs, v2, base0 + (head + 1) * hstride, 0);
        v[2] = padv; v[3] = 0u;
      }
      stg(rs, v, base0 + head * hstride + (cc >> 1) * 1024 + (cc & 1) * 512, 0);
    }
  }
}

template <bool V1, bool V2>
DI void p1_sweep1(const P1Ctx& c, char* lds, bf16x8 (&xf)[28], f32x16& acc0, f32x16& acc1, int wl) {
  const int lane = c.lane, r32 = lane & 31, hh = lane >> 5;
  const int xrow = (c.wid & 3) * 32 + r32, xsw = (xrow >> 1) & 7;
  lds_cptr xb = (lds_cptr)lds + L_X + xrow * 128;
  bf16x8 wf[2][8];
#pragma unroll
  for (int ch = 0; ch < 8; ++ch) {
    if (ch == 0) WAITBAR(8);
    else if (ch <= 3) WAITBAR(6);
    else if (ch <= 5) WAITBAR(10);
    else if (ch == 6) WAITBAR(3);
    else WAITBAR(0);
    if (ch == 0) STAMP(9);
    if (ch == 3) STAMP(10);
    if (ch == 6) STAMP(11);
    lds_cptr wb = (lds_cptr)lds + L_WR + (ch % 3) * 16384 + wl;
    lds_cptr xc = xb + (ch == 6 ? 0 : ch) * 16384;
#pragma unroll
    for (int s = 0; s < 4; ++s) {
      if (ch < 7) {
        xf[ch * 4 + s] = ldsrd16(xc + (((2 * s + hh) ^ xsw) * 16));
        lds_cptr wp = wb + s * 2048;
        wf[ch & 1][s] = cat8(trrd(wp), trrd(wp + 512));
        wf[ch & 1][4 + s] = cat8(trrd(wp + 8192), trrd(wp + 8192 + 512));
      }
      if (ch >= 1) {
        const bf16x8 x8 = xf[(ch - 1) * 4 + s];
        const bf16x8 w0 = wf[(ch - 1) & 1][s], w1 = wf[(ch - 1) & 1][4 + s];
        acc0 = V1 ? MFMA(x8, w0, acc0) : MFMA(w0, x8, acc0);
        acc1 = V2 ? MFMA(x8, w1, acc1) : MFMA(w1, x8, acc1);
      }
      __builtin_amdgcn_sched_barrier(0);
      if (s == 0 && ch >= 1 && ch <= 4) p1_w12(c, ch + 2);
      if (s == 1 && ch <= 3) p1_x(c, ch + 3);
      if (s == 2 && ch == 3) { p1_w3(c, 0); p1_w3(c, 1); }
      if (s == 3 && ch == 3) { p1_w3(c, 2); p1_w3(c, 3); }
      if (s == 1 && ch == 4) { p1_w3(c, 4); p1_w3(c, 5); }
      if (s == 1 && ch == 5) { p1_w3(c, 6); }
      __builtin_amdgcn_sched_barrier(0);
    }
  }
}
template <bool V3>
DI void p1_sweep2(char* lds, const bf16x8 (&xf)[28], f32x16& acc0, int wl) {
#pragma unroll
  for (int ch = 0; ch < 7; ++ch) {
    lds_cptr wb = (lds_cptr)lds + w3_off(ch) + wl;
#pragma unroll
    for (int s = 0; s < 4; ++s) {
      lds_cptr wp = wb + s * 2048;
      const bf16x8 w0 = cat8(trrd(wp), trrd(wp + 512));
      acc0 = V3 ? MFMA(xf[ch * 4 + s], w0, acc0) : MFMA(w0, xf[ch * 4 + s], acc0);
    }
  }
}

__global__ __launch_bounds__(512, 1) void fused_k(const u16* __restrict__ x, const u16* __restrict__ wA,
                                                  const u16* __restrict__ wB, const u16* __restrict__ woA,
                                                  const u16* __restrict__ woB, const u16* __restrict__ gw,
                                                  char* ws, u16* __restrict__ out) {
  extern __shared__ __attribute__((aligned(16))) char lds[];
  const int tid = threadIdx.x, lane = tid & 63;
  const int wid = __builtin_amdgcn_readfirstlane(tid >> 6);
  const int b = blockIdx.x;
  const int r32 = lane & 31, hh = lane >> 5;
  const int g16 = lane >> 4, i16 = lane & 15, q4 = i16 >> 2, p4 = i16 & 3, cb = g16 & 1;
  const unsigned lds0 = (unsigned)(uintptr_t)lds;
  const __amdgpu_buffer_rsrc_t rs = __builtin_amdgcn_make_buffer_rsrc((void*)ws, 0, (int)WS_END, 0x00020000);
  const u64 tag = ((gm_dispatch_id() + 1) * 0x9E3779B97F4A7C15ull) ^ (u64)(uintptr_t)__builtin_amdgcn_dispatch_ptr() ^ 0x5A17C3E9D2B40F61ull;
  const f32x16 zero = {};
  const unsigned l16 = lane * 16;
  STAMP(0);
  const int h = (b & 7) + 8 * ((b >> 3) & 1), rb = b >> 4;
  unsigned fb_off = 0;
  if (tid == 0) *(__attribute__((address_space(3))) unsigned*)((lds_ptr)lds + L_CNT) = 0u;

  if (b < 224) {
    const int i5 = b >> 3, g = 7 * (b & 1) + i5 % 7, rb1 = 4 * ((b & 7) >> 1) + i5 / 7;
    const int w1 = g < 7 ? 2 * g : 7 + g, w2 = g < 7 ? 2 * g + 1 : 2 * g - 7, w3 = g < 7 ? g : 2 * g - 6;
    const int br2 = g < 7 ? 0 : 1;
    P1Ctx c;
    c.x = (const char*)x; c.lds0 = lds0; c.wid = wid; c.lane = lane;
    c.rot = (i5 % 7 + 2 * (i5 / 7)) % 7;
    c.w1 = (const char*)wB + w1 * 128;
    c.w2 = (const char*)(g < 7 ? wB : wA) + w2 * 128;
    c.w3 = (const char*)wA + w3 * 128;
    {
      const int rl8 = lane >> 3, row0 = 8 * wid + rl8, row1 = row0 + 64;
      c.xoff0 = (unsigned)((rb1 * 128 + row0) * 896 + (((lane & 7) ^ ((row0 >> 1) & 7)) * 16));
      c.xoff1 = (unsigned)((rb1 * 128 + row1) * 896 + (((lane & 7) ^ ((row1 >> 1) & 7)) * 16));
      c.woff = (unsigned)(row0 * 2688 + (((lane & 7) ^ (((rl8 >> 1) & 1) << 2)) * 16));
    }
    if (wid == 7 && lane < 56) glds16(gw, lane * 16, lds0 + L_GW);
    p1_x(c, 0); p1_w12(c, 0);
    asm volatile("s_barrier" ::: "memory");
    p1_x(c, 1); p1_w12(c, 1); p1_x(c, 2); p1_w12(c, 2);
    const int rt = wid & 3, ct = wid >> 2, t64 = rb1 * 4 + rt;
    const int wl = (8 * hh + q4) * 128 + ((ct ^ (q4 >> 1)) * 64 + cb * 32 + p4 * 8);
    bf16x8 xf[28];
    f32x16 acc0 = zero, acc1 = zero;
    if (g < 7) {
      p1_sweep1<false, false>(c, lds, xf, acc0, acc1, wl);
      p1_store<false>(acc0, 0, w1, ct, t64, lane, rs);
      p1_store<false>(acc1, br2, w2, ct, t64, lane, rs);
    } else if (g < 11) {
      p1_sweep1<true, false>(c, lds, xf, acc0, acc1, wl);
      p1_store<true>(acc0, 0, w1, ct, t64, lane, rs);
      p1_store<false>(acc1, br2, w2, ct, t64, lane, rs);
    } else {
      p1_sweep1<true, true>(c, lds, xf, acc0, acc1, wl);
      p1_store<true>(acc0, 0, w1, ct, t64, lane, rs);
      p1_store<true>(acc1, br2, w2, ct, t64, lane, rs);
    }
    STAMP(1);
    acc0 = zero;
    if (g < 10) p1_sweep2<false>(lds, xf, acc0, wl); else p1_sweep2<true>(lds, xf, acc0, wl);
    float gt = 0.f;
    if (wid == g + 4) {
      float z = 0.f;
      lds_cptr gp = (lds_cptr)lds + L_GW + hh * 16;
#pragma unroll
      for (int k = 0; k < 28; ++k) {
        const u32x4 gv = __builtin_bit_cast(u32x4, ldsrd16(gp + rotc(c, k >> 2) * 128 + (k & 3) * 32));
        const u32x4 xv = __builtin_bit_cast(u32x4, xf[k]);
#pragma unroll
        for (int e = 0; e < 4; ++e) {
          const unsigned xe = xv[e], ge = gv[e];
          z = __builtin_amdgcn_fdot2_f32_bf16(__builtin_bit_cast(bf2_t, xe), __builtin_bit_cast(bf2_t, ge), z, false);
        }
      }
      z = xhalf_sum(z);
      gt = 1.0f / (1.0f + __expf(-z));
    }
    STAMP(2);
    WAITBAR(0);
    if (tid == 0) set_flag(ws, WS_FA + (g * 16 + rb1) * 128, tag);
    STAMP(13);
    if (wid == 0) poll_fa(ws, lane, h, rb, tag);
    if (g < 10) p1_store<false>(acc0, 1, w3, ct, t64, lane, rs); else p1_store<true>(acc0, 1, w3, ct, t64, lane, rs);
    if (wid == g + 4 && hh == 0) __builtin_amdgcn_raw_buffer_store_b32(__float_as_uint(gt), rs, r32 * 4, WS_G + t64 * 128, 16);
    fb_off = WS_FB + (g * 16 + rb1) * 128;
  } else {
    if (wid == 0) poll_fa(ws, lane, h, rb, tag);
  }
  STAMP(3);

  BARRIER();
  STAMP(4);
  f32x16 o0 = zero, o1 = zero, o2 = zero, o3 = zero;
  {
    const unsigned kvh = WS_KV + (h * 64) * 4096;
    const unsigned qb = WS_QF + (h * 64 + rb * 4) * 2048;
    bf16x8 q00 = ldg(rs, l16, qb), q01 = ldg(rs, l16 + 1024, qb);
    bf16x8 q10 = ldg(rs, l16 + 2048, qb), q11 = ldg(rs, l16 + 3072, qb);
    bf16x8 q20 = ldg(rs, l16, qb + 4096), q21 = ldg(rs, l16 + 1024, qb + 4096);
    bf16x8 q30 = ldg(rs, l16 + 2048, qb + 4096), q31 = ldg(rs, l16 + 3072, qb + 4096);
    const bf16x8 kr0 = ldg(rs, l16, kvh), kr1 = ldg(rs, l16 + 1024, kvh);
    const unsigned kvw = kvh + wid * 8 * 4096;
#define TADDR(t) (kvw + (unsigned)((((t) + rb) & 7) * 4096))
    bf16x8 ka0 = ldg(rs, l16, TADDR(0)), ka1 = ldg(rs, l16 + 1024, TADDR(0));
    bf16x8 kb0 = ldg(rs, l16, TADDR(1)), kb1 = ldg(rs, l16 + 1024, TADDR(1));
    bf16x8 va0 = ldg(rs, l16 + 2048, TADDR(0)), va1 = ldg(rs, l16 + 3072, TADDR(0));
    bf16x8 kc0 = ldg(rs, l16, TADDR(2)), kc1 = ldg(rs, l16 + 1024, TADDR(2));
    bf16x8 vb0 = ldg(rs, l16 + 2048, TADDR(1)), vb1 = ldg(rs, l16 + 3072, TADDR(1));
    bf16x8 vc0, vc1;
    asm volatile("s_waitcnt vmcnt(10)" ::: "memory");
    if (fb_off && lane == 0) {
      const unsigned old = __hip_atomic_fetch_add((__attribute__((address_space(3))) unsigned*)((lds_ptr)lds + L_CNT), 1u, __ATOMIC_RELAXED, __HIP_MEMORY_SCOPE_WORKGROUP);
      if (old == 7) set_flag(ws, fb_off, tag);
    }
    if (wid >= 4) __builtin_amdgcn_s_setprio(1);
    const bool odd = h & 1;
    const bool mylane = odd ? !hh : hh;
#define SETM(qa, qb_)                                                                   \
  {                                                                                     \
    f32x16 s = MFMA(kr0, qa, zero); s = MFMA(kr1, qb_, s);                              \
    const unsigned nm = pk2(-xhalf_max(max16(s)), 0.f);                                 \
    u32x4 ta = __builtin_bit_cast(u32x4, qa), tb = __builtin_bit_cast(u32x4, qb_);      \
    if (mylane) { if (odd) ta[0] = nm; else tb[2] = nm; }                               \
    qa = __builtin_bit_cast(bf16x8, ta); qb_ = __builtin_bit_cast(bf16x8, tb);          \
  }
    SETM(q00, q01) SETM(q10, q11) SETM(q20, q21) SETM(q30, q31)
#undef SETM
    f32x16 s0 = MFMA(ka0, q00, zero); s0 = MFMA(ka1, q01, s0);
    f32x16 s1 = MFMA(ka0, q10, zero); s1 = MFMA(ka1, q11, s1);
    f32x16 s2 = MFMA(ka0, q20, zero); s2 = MFMA(ka1, q21, s2);
    f32x16 s3 = zero;
    u32x4 pa, pb0, pb1, pb2, pb3;
    STAMP(14);
#define SB() __builtin_amdgcn_sched_barrier(0)
#define PIN(x) asm volatile("" : "+v"(x))
#define EX4(S, i) { S[i] = __builtin_amdgcn_exp2f(S[i]); S[i + 1] = __builtin_amdgcn_exp2f(S[i + 1]); S[i + 2] = __builtin_amdgcn_exp2f(S[i + 2]); S[i + 3] = __builtin_amdgcn_exp2f(S[i + 3]); }
#define BLK(S, O, PB, V0, PS, PO, PPB, PV1, PK0, PK1, PQA, PQB, DOPV, DOQK)              \
  {                                                                                      \
    if (DOPV) PO = MFMA(PV1, __builtin_bit_cast(bf16x8, PPB), PO);                       \
    EX4(S, 0) PIN(S); SB();                                                              \
    if (DOQK) PS = MFMA(PK0, PQA, zero);                                                 \
    EX4(S, 4) pa = pack8u(S, 0); PIN(S); PIN(pa); SB();                                  \
    if (DOQK) PS = MFMA(PK1, PQB, PS);                                                   \
    EX4(S, 8) PIN(S); SB();                                                              \
    O = MFMA(V0, __builtin_bit_cast(bf16x8, pa), O);                                     \
    EX4(S, 12) PB = pack8u(S, 8); PIN(PB); SB();                                         \
  }
#define STEP(t, V0, V1, K0, K1, PV0, PV1, PK0, PK1)                                      \
  {                                                                                      \
    BLK(s0, o0, pb0, V0, s3, o3, pb3, PV1, PK0, PK1, q30, q31, t > 0, true)              \
    if (t + 3 < 8) { PK0 = ldg(rs, l16, TADDR(t + 3)); PK1 = ldg(rs, l16 + 1024, TADDR(t + 3)); }        \
    if (t + 2 < 8) { PV0 = ldg(rs, l16 + 2048, TADDR(t + 2)); PV1 = ldg(rs, l16 + 3072, TADDR(t + 2)); } \
    SB();                                                                                \
    BLK(s1, o1, pb1, V0, s0, o0, pb0, V1, K0, K1, q00, q01, true, t < 7)                 \
    BLK(s2, o2, pb2, V0, s1, o1, pb1, V1, K0, K1, q10, q11, true, t < 7)                 \
    BLK(s3, o3, pb3, V0, s2, o2, pb2, V1, K0, K1, q20, q21, true, t < 7)                 \
  }
    STEP(0, va0, va1, kb0, kb1, vc0, vc1, ka0, ka1)
    STEP(1, vb0, vb1, kc0, kc1, va0, va1, kb0, kb1)
    STEP(2, vc0, vc1, ka0, ka1, vb0, vb1, kc0, kc1)
    STEP(3, va0, va1, kb0, kb1, vc0, vc1, ka0, ka1)
    STEP(4, vb0, vb1, kc0, kc1, va0, va1, kb0, kb1)
    STEP(5, vc0, vc1, ka0, ka1, vb0, vb1, kc0, kc1)
    STEP(6, va0, va1, kb0, kb1, vc0, vc1, ka0, ka1)
    STEP(7, vb0, vb1, kc0, kc1, va0, va1, kb0, kb1)
    o3 = MFMA(vb1, __builtin_bit_cast(bf16x8, pb3), o3);
#undef TADDR
#undef BLK
#undef EX4
#undef STEP
  }
  STAMP(5);
  STAMPW(12, 4);
  {
    typedef __attribute__((address_space(3))) f32x4* lf4;
    lds_ptr cbp = (lds_ptr)lds + L_CB + wid * 2048 + l16;
#define WRQ(dst, O, o8)                                                                 \
  {                                                                                     \
    lds_ptr p = cbp + (dst) * 16384;                                                    \
    *(lf4)p = (f32x4){O[o8], O[o8 + 1], O[o8 + 2], O[o8 + 3]};                          \
    *(lf4)(p + 1024) = (f32x4){O[o8 + 4], O[o8 + 5], O[o8 + 6], O[o8 + 7]};             \
  }
    WRQ(0, o0, 0) WRQ(1, o0, 8) WRQ(2, o1, 0) WRQ(3, o1, 8) WRQ(4, o2, 0) WRQ(5, o2, 8) WRQ(6, o3, 0) WRQ(7, o3, 8)
#undef WRQ
    __attribute__((address_space(3))) float* lb = (__attribute__((address_space(3))) float*)((lds_ptr)lds + L_LB);
    if (hh) {
      lb[wid * 32 + r32] = o0[12]; lb[256 + wid * 32 + r32] = o1[12];
      lb[512 + wid * 32 + r32] = o2[12]; lb[768 + wid * 32 + r32] = o3[12];
    }
  }
  if (wid >= 4) {
        {
          const int pr = rb > 0 ? rb - 1 : rb;
          const int cw = (lane & 1) ? (28 * h + 27) >> 6 : (28 * h) >> 6;
          const int kind = lane >> 1;
          const int gg = kind == 0 ? cw : (kind & 1) ? 7 + (cw >> 1) : 7 + ((7 + cw) >> 1);
          wait_flags(ws, WS_FB + (gg * 16 + (kind >= 3 ? pr : rb)) * 128, lane < 10, tag);
        }
        const int t64 = rb * 4 + wid - 4;
        const unsigned qo = WS_QF + ((16 + h) * 64 + t64) * 2048;
        const unsigned kc = WS_KV + ((16 + h) * 64 + t64) * 4096;
        const unsigned kp = t64 > 0 ? kc - 4096 : kc;
        const bf16x8 q0 = ldg(rs, l16, qo), q1 = ldg(rs, l16 + 1024, qo);
        const bf16x8 kc0 = ldg(rs, l16, kc), kc1 = ldg(rs, l16 + 1024, kc), vc0 = ldg(rs, l16 + 2048, kc), vc1 = ldg(rs, l16 + 3072, kc);
        const bf16x8 kp0 = ldg(rs, l16, kp), kp1 = ldg(rs, l16 + 1024, kp), vp0 = ldg(rs, l16 + 2048, kp), vp1 = ldg(rs, l16 + 3072, kp);
        f32x16 sc = MFMA(kc0, q0, zero); sc = MFMA(kc1, q1, sc);
        f32x16 sp = MFMA(kp0, q0, zero); sp = MFMA(kp1, q1, sp);
#pragma unroll
        for (int r = 0; r < 16; ++r) {
          const int i = (r & 3) + 8 * (r >> 2) + 4 * hh;
          const int dc = r32 - i, dp = r32 + 32 - i;
          const bool vc = (dc >= 0) && (dc <= 14) && !(dc & 1);
          const bool vp = (dp <= 14) && !(dp & 1) && (t64 > 0);
          sc[r] = vc ? sc[r] : -INFINITY;
          sp[r] = vp ? sp[r] : -INFINITY;
        }
        const float m = xhalf_max(fmaxf(max16(sc), max16(sp)));
#pragma unroll
        for (int r = 0; r < 16; ++r) {
          sc[r] = __builtin_amdgcn_exp2f(sc[r] - m);
          sp[r] = __builtin_amdgcn_exp2f(sp[r] - m);
        }
        f32x16 o = MFMA(vc0, pack8(sc, 0), zero);
        o = MFMA(vc1, pack8(sc, 8), o);
        o = MFMA(vp0, pack8(sp, 0), o);
        o = MFMA(vp1, pack8(sp, 8), o);
        const float l = __shfl(o[12], 32 + r32);
        const float rl = 1.0f / l;
#pragma unroll
        for (int r = 0; r < 16; ++r) o[r] *= rl;
        if (hh) { o[12] = 0.f; o[13] = 0.f; o[14] = 0.f; o[15] = 0.f; }
        const unsigned od = WS_OF + ((64 + t64) * 16 + h) * 2048;
        stg(rs, pack8u(o, 0), l16, od);
        stg(rs, pack8u(o, 8), l16 + 1024, od);
        asm volatile("s_waitcnt vmcnt(0)" ::: "memory");
        if (lane == 0) set_flag(ws, WS_F3A + (t64 * 16 + h) * 128, tag);
      }
  BARRIER();
  STAMP(15);
  {
    typedef __attribute__((address_space(3))) const f32x4* lf4c;
    lds_cptr p = (lds_cptr)lds + L_CB + wid * 16384 + l16;
    f32x4 u = *(lf4c)p, w = *(lf4c)(p + 1024);
#pragma unroll
    for (int src = 1; src < 8; ++src) {
      u += *(lf4c)(p + src * 2048);
      w += *(lf4c)(p + src * 2048 + 1024);
    }
    const __attribute__((address_space(3))) float* lb =
        (const __attribute__((address_space(3))) float*)((lds_cptr)lds + L_LB) + (wid >> 1) * 256 + r32;
    float l = 0.f;
#pragma unroll
    for (int src = 0; src < 8; ++src) l += lb[src * 32];
    const float rl = 1.0f / l;
    u *= rl; w *= rl;
    if (hh && (wid & 1)) w = (f32x4){0.f, 0.f, 0.f, 0.f};
    const u32x4 pkd = {pk2(u[0], u[1]), pk2(u[2], u[3]), pk2(w[0], w[1]), pk2(w[2], w[3])};
    const int t64 = rb * 4 + (wid >> 1);
    stg(rs, pkd, l16, WS_OF + (t64 * 16 + h) * 2048 + (wid & 1) * 1024);
  }
  WAITBAR(0);
  if (tid == 0) set_flag(ws, WS_F3 + (rb * 16 + h) * 128, tag);
  STAMP(6);
  if (b >= 224) { STAMPDUMP(); return; }

  {
    const int i5 = b >> 3, rg = (b & 7) + 8 * (i5 / 7), cg3 = i5 % 7;
    {
      const int rl8 = lane >> 3;
      const unsigned vo = (unsigned)((wid * 8 + rl8) * 896 + cg3 * 128 + (((lane & 7) ^ (((rl8 >> 1) & 1) << 2)) * 16));
#pragma unroll
      for (int i = 0; i < 7; ++i) glds16((const char*)woB + i * 64 * 896, vo, lds0 + L_WO + (i * 8 + wid) * 1024);
#pragma unroll
      for (int i = 0; i < 7; ++i) glds16((const char*)woA + i * 64 * 896, vo, lds0 + L_WO + WO_BR + (i * 8 + wid) * 1024);
      if (tid < 64) {
        typedef __attribute__((address_space(3))) u32x4* lu4;
        *(lu4)((lds_ptr)lds + L_WO + (tid >> 5) * WO_BR + 448 * 128 + (tid & 31) * 16) = (u32x4){0u, 0u, 0u, 0u};
      }
    }
    if (wid == 0) {
      const unsigned off = lane < 32 ? WS_F3A + (rg * 32 + lane) * 128 : WS_F3 + ((rg >> 1) * 16 + lane - 32) * 128;
      wait_flags(ws, off, lane < 48, tag);
    }
    BARRIER();
    STAMP(7);
    const int rt = wid & 1, br = (wid >> 1) & 1, kh = wid >> 2;
    const unsigned ob = WS_OF + ((br * 64 + rg * 2 + rt) * 16 + kh * 8) * 2048;
    bf16x8 of[16];
#pragma unroll
    for (int i = 0; i < 16; ++i) of[i] = ldg(rs, l16 + (i & 3) * 1024, ob + (i >> 2) * 4096);
    const float g = __uint_as_float(__builtin_amdgcn_raw_buffer_load_b32(rs, r32 * 4, WS_G + (rg * 2 + rt) * 128, 16));
    WAITBAR(17);
    lds_cptr wp0 = (lds_cptr)lds + L_WO + br * WO_BR + (kh * 8 * 28 + 4 * hh + q4) * 128 + cb * 32 + p4 * 8;
    const int sw = (q4 >> 1) * 64;
    f32x16 acc0 = zero, acc1 = zero;
#pragma unroll
    for (int hd = 0; hd < 8; ++hd) {
#pragma unroll
      for (int s = 0; s < 2; ++s) {
        lds_cptr p = wp0 + (hd * 28 + 16 * s) * 128;
        const bf16x8 wf0 = cat8(trrd(p + sw), trrd(p + sw + 8 * 128));
        const bf16x8 wf1 = cat8(trrd(p + (64 - sw)), trrd(p + (64 - sw) + 8 * 128));
        acc0 = MFMA(wf0, of[hd * 2 + s], acc0);
        acc1 = MFMA(wf1, of[hd * 2 + s], acc1);
      }
    }
    const float gs = br ? g : 1.f - g;
#pragma unroll
    for (int r = 0; r < 16; ++r) { acc0[r] *= gs; acc1[r] *= gs; }
    BARRIER();
    typedef __attribute__((address_space(3))) f32x4* lf4;
    const int qd = wid >> 1;
    lds_ptr ab = (lds_ptr)lds + rt * 32768 + qd * 2048 + l16;
#define WRQ(dst, A, o8)                                                                 \
  {                                                                                     \
    lds_ptr p = ab + (dst) * 8192;                                                      \
    *(lf4)p = (f32x4){A[o8], A[o8 + 1], A[o8 + 2], A[o8 + 3]};                          \
    *(lf4)(p + 1024) = (f32x4){A[o8 + 4], A[o8 + 5], A[o8 + 6], A[o8 + 7]};             \
  }
    WRQ(0, acc0, 0) WRQ(1, acc0, 8) WRQ(2, acc1, 0) WRQ(3, acc1, 8)
#undef WRQ
    BARRIER();
    {
      lds_ptr p = (lds_ptr)lds + rt * 32768 + qd * 8192 + l16;
      f32x4 u = *(lf4)p, w = *(lf4)(p + 1024);
#pragma unroll
      for (int src = 1; src < 4; ++src) { u += *(lf4)(p + src * 2048); w += *(lf4)(p + src * 2048 + 1024); }
      const int row = rg * 64 + rt * 32 + r32;
      const int col = cg3 * 64 + (qd >> 1) * 32 + (qd & 1) * 16 + 4 * hh;
      *(u32x2*)(out + (size_t)row * 448 + col) = (u32x2){pk2(u[0], u[1]), pk2(u[2], u[3])};
      *(u32x2*)(out + (size_t)row * 448 + col + 8) = (u32x2){pk2(w[0], w[1]), pk2(w[2], w[3])};
    }
  }
  STAMP(8);
  STAMPDUMP();
}

extern "C" void kernel_launch(void* const* d_in, const int* in_sizes, int n_in, void* d_out, int out_size,
                              void* d_ws, size_t ws_size, hipStream_t stream) {
  const u16* x = (const u16*)d_in[0];
  const u16* wqkvA = (const u16*)d_in[1];
  const u16* woA = (const u16*)d_in[2];
  const u16* wqkvB = (const u16*)d_in[4];
  const u16* woB = (const u16*)d_in[5];
  const u16* gw = (const u16*)d_in[6];
  (void)hipFuncSetAttribute((const void*)fused_k, hipFuncAttributeMaxDynamicSharedMemorySize, LDS_BYTES);
  hipLaunchKernelGGL(fused_k, dim3(256), dim3(512), LDS_BYTES, stream, x, wqkvA, wqkvB, woA, woB, gw, (char*)d_ws, (u16*)d_out);
}
```

```cpp
#include <hip/hip_runtime.h>
#include <hip/hip_bf16.h>
#include <stdint.h>

typedef unsigned short u16;
typedef unsigned long long u64;
typedef short bf16x8 __attribute__((ext_vector_type(8)));
typedef short s16x4 __attribute__((ext_vector_type(4)));
typedef float f32x16 __attribute__((ext_vector_type(16)));
typedef float f32x2 __attribute__((ext_vector_type(2)));
typedef float f32x4 __attribute__((ext_vector_type(4)));
typedef unsigned u32x4 __attribute__((ext_vector_type(4)));
typedef unsigned u32x2 __attribute__((ext_vector_type(2)));
typedef __bf16 bf2_t __attribute__((ext_vector_type(2)));
typedef __attribute__((address_space(3))) const char* lds_cptr;
typedef __attribute__((address_space(3))) char* lds_ptr;
typedef __attribute__((address_space(1))) u64 gu64;
typedef short v4i16_t __attribute__((ext_vector_type(4)));

#define DI __device__ __forceinline__
#define POLL_SLEEP 10
#define STAMP(i)
#define STAMPW(i, w)
#define STAMPX(i)
#define STAMPDUMP()
#define MFMA(a, b, c) __builtin_amdgcn_mfma_f32_32x32x16_bf16((a), (b), (c), 0, 0, 0)
#define BARRIER() asm volatile("s_waitcnt lgkmcnt(0)\n\ts_barrier" ::: "memory")
#define WAITBAR(N) asm volatile("s_waitcnt vmcnt(" #N ") lgkmcnt(0)\n\ts_barrier" ::: "memory")

extern "C" __device__ u64 gm_dispatch_id() __asm("llvm.amdgcn.dispatch.id");

#define CODE_LINES 264
constexpr float QSCALE = 0.18898223650461362f * 1.4426950408889634f;

constexpr unsigned WS_QF = 0;
constexpr unsigned WS_KV = 4u << 20;
constexpr unsigned WS_OF = 12u << 20;
constexpr unsigned WS_G = 16u << 20;
constexpr unsigned WS_FA = WS_G + 65536;
constexpr unsigned WS_FB = WS_FA + 32768;
constexpr unsigned WS_F3 = WS_FB + 32768;
constexpr unsigned WS_F3A = WS_F3 + 32768;
constexpr unsigned WS_END = WS_F3A + 131072;

constexpr int LDS_BYTES = 163840;
constexpr int L_X = 0;
constexpr int L_WR = 98304;
constexpr int L_GW = 147456;
constexpr int L_CNT = 163328;
constexpr int L_CB = 0;
constexpr int L_WOA = 65536;
constexpr int L_WOB1 = 123392;

DI unsigned pk2(float a, float b) {
  f32x2 v = {a, b};
  bf2_t r = __builtin_convertvector(v, bf2_t);
  return __builtin_bit_cast(unsigned, r);
}
DI u32x4 pack8u(const f32x16& x, int o) {
  u32x4 p;
  p[0] = pk2(x[o + 0], x[o + 1]); p[1] = pk2(x[o + 2], x[o + 3]);
  p[2] = pk2(x[o + 4], x[o + 5]); p[3] = pk2(x[o + 6], x[o + 7]);
  return p;
}
DI bf16x8 pack8(const f32x16& x, int o) { return __builtin_bit_cast(bf16x8, pack8u(x, o)); }
DI void glds16(const void* sbase, unsigned voff, unsigned lds_base) {
  unsigned sv;
  asm volatile("s_mov_b32 %0, m0\n\ts_mov_b32 m0, %3\n\ts_nop 4\n\tglobal_load_lds_dwordx4 %1, %2\n\ts_mov_b32 m0, %0"
               : "=&s"(sv) : "v"(voff), "s"(sbase), "s"(lds_base) : "memory");
}
DI s16x4 trrd(lds_cptr p) {
  return __builtin_bit_cast(s16x4, __builtin_amdgcn_ds_read_tr16_b64_v4i16((__attribute__((address_space(3))) v4i16_t*)p));
}
DI bf16x8 cat8(s16x4 lo, s16x4 hi) { return __builtin_shufflevector(lo, hi, 0, 1, 2, 3, 4, 5, 6, 7); }
DI bf16x8 ldsrd16(lds_cptr p) { return *(const __attribute__((address_space(3))) bf16x8*)p; }
DI float xhalf_max(float m) {
  auto rr = __builtin_amdgcn_permlane32_swap(__float_as_uint(m), __float_as_uint(m), false, false);
  return fmaxf(__uint_as_float(rr[0]), __uint_as_float(rr[1]));
}
DI float xhalf_sum(float m) {
  auto rr = __builtin_amdgcn_permlane32_swap(__float_as_uint(m), __float_as_uint(m), false, false);
  return __uint_as_float(rr[0]) + __uint_as_float(rr[1]);
}
DI float max16(const f32x16& s) {
  float a = fmaxf(fmaxf(s[0], s[1]), s[2]), b = fmaxf(fmaxf(s[3], s[4]), s[5]);
  a = fmaxf(fmaxf(a, s[6]), s[7]); b = fmaxf(fmaxf(b, s[8]), s[9]);
  a = fmaxf(fmaxf(a, s[10]), s[11]); b = fmaxf(fmaxf(b, s[12]), s[13]);
  a = fmaxf(fmaxf(a, s[14]), s[15]);
  return fmaxf(a, b);
}
DI bf16x8 ldg(__amdgpu_buffer_rsrc_t rs, unsigned voff, unsigned soff) {
  return __builtin_bit_cast(bf16x8, __builtin_amdgcn_raw_buffer_load_b128(rs, voff, soff, 16));
}
DI void stg(__amdgpu_buffer_rsrc_t rs, u32x4 v, unsigned voff, unsigned soff) {
  __builtin_amdgcn_raw_buffer_store_b128(v, rs, voff, soff, 16);
}
DI void wait_flags(const char* ws, unsigned off, bool active, u64 tag) {
  gu64* p = (gu64*)(ws + off);
  const u64 t0 = __builtin_amdgcn_s_memrealtime();
  for (;;) {
    u64 v = tag;
    if (active) v = __hip_atomic_load(p, __ATOMIC_RELAXED, __HIP_MEMORY_SCOPE_AGENT);
    if (__all(v == tag)) break;
    __builtin_amdgcn_s_sleep(1);
    if (__builtin_amdgcn_s_memrealtime() - t0 > 5000000ull) break;
  }
  __builtin_amdgcn_fence(__ATOMIC_ACQUIRE, "wavefront");
}
DI void set_flag(char* ws, unsigned off, u64 tag) {
  __hip_atomic_store((gu64*)(ws + off), tag, __ATOMIC_RELAXED, __HIP_MEMORY_SCOPE_AGENT);
}

DI void poll_fa(const char* ws, int lane, int wid, int h, int rb, u64 tag) {
  const int cw = (lane & 1) ? (28 * h + 27) >> 6 : (28 * h) >> 6;
  const int kind = lane >> 1;
  const int g = kind == 5 ? cw >> 1 : (kind == 2 || kind == 3) ? 7 + cw : (7 + cw) >> 1;
  const int r = kind == 5 ? rb : kind == 4 ? 0 : 2 * wid + (kind & 1);
  wait_flags(ws, WS_FA + (g * 16 + r) * 128, lane < 12, tag);
}

typedef __attribute__((address_space(3))) unsigned* lds_u32p;
DI unsigned lds_arrive(char* lds, int idx) {
  return __hip_atomic_fetch_add((lds_u32p)((lds_ptr)lds + L_CNT) + idx, 1u, __ATOMIC_RELAXED, __HIP_MEMORY_SCOPE_WORKGROUP);
}
DI void lds_wait(char* lds, int idx, unsigned n) {
  while (__hip_atomic_load((lds_u32p)((lds_ptr)lds + L_CNT) + idx, __ATOMIC_RELAXED, __HIP_MEMORY_SCOPE_WORKGROUP) < n) __builtin_amdgcn_s_sleep(1);
  asm volatile("" ::: "memory");
}

struct P1Ctx {
  const char* x; const char* w1; const char* w2; const char* w3;
  unsigned lds0, xoff0, xoff1, woff;
  int wid, lane, rot;
};
DI int rotc(const P1Ctx& c, int ch) { const int t = ch + c.rot; return t >= 7 ? t - 7 : t; }
DI void p1_x(const P1Ctx& c, int ch) {
  const int slot = ch == 6 ? 0 : ch;
  const char* src = c.x + rotc(c, ch) * 128;
  glds16(src, c.xoff0, c.lds0 + L_X + slot * 16384 + c.wid * 1024);
  glds16(src, c.xoff1, c.lds0 + L_X + slot * 16384 + (c.wid + 8) * 1024);
}
DI void p1_w12(const P1Ctx& c, int ch) {
  const int slot = ch % 3;
  const int ro = rotc(c, ch) * (64 * 2688);
  glds16(c.w1 + ro, c.woff, c.lds0 + L_WR + slot * 16384 + c.wid * 1024);
  glds16(c.w2 + ro, c.woff, c.lds0 + L_WR + slot * 16384 + 8192 + c.wid * 1024);
}
DI constexpr int w3_off(int ch) { return L_X + (1 + (ch >> 1)) * 16384 + (ch & 1) * 8192; }
DI void p1_w3(const P1Ctx& c, int ch) {
  glds16(c.w3 + rotc(c, ch) * (64 * 2688), c.woff, c.lds0 + w3_off(ch) + c.wid * 1024);
}

template <bool ISV>
DI void p1_store(const f32x16& a, int br, int w, int ct, int t64, int lane, __amdgpu_buffer_rsrc_t rs) {
  const int r32 = lane & 31, hh = lane >> 5;
  const int sec = w / 7, wc = (w - sec * 7) * 64;
  if (ISV) {
    const int c = wc + ct * 32 + r32, head = c / 28, d = c - head * 28;
    const unsigned off = WS_KV + ((br * 16 + head) * 64 + t64) * 4096 + 2048 + (hh * 32 + d) * 16;
    stg(rs, pack8u(a, 0), off, 0);
    stg(rs, pack8u(a, 8), off + 1024, 0);
    if (d >= 24) {
      const unsigned f = d == 24 ? 0x3F803F80u : 0u;
      const u32x4 pv = {f, f, f, f};
      stg(rs, pv, off + 64, 0);
      stg(rs, pv, off + 64 + 1024, 0);
    }
  } else {
    const float sc = sec == 0 ? QSCALE : 1.f;
    unsigned w8[8];
#pragma unroll
    for (int i = 0; i < 8; ++i) w8[i] = pk2(a[2 * i] * sc, a[2 * i + 1] * sc);
    const unsigned padv = sec == 0 ? 0u : 0x3F80u;
    const unsigned hstride = sec == 0 ? 64u * 2048u : 64u * 4096u;
    const unsigned base0 = (sec == 0 ? WS_QF + (br * 16 * 64 + t64) * 2048 : WS_KV + (br * 16 * 64 + t64) * 4096) + r32 * 16;
#pragma unroll
    for (int pp = 0; pp < 2; ++pp) {
      const auto s0 = __builtin_amdgcn_permlane32_swap(w8[4 * pp], w8[4 * pp + 2], false, false);
      const auto s1 = __builtin_amdgcn_permlane32_swap(w8[4 * pp + 1], w8[4 * pp + 3], false, false);
      const int C = (wc >> 3) + ct * 4 + 2 * pp + hh, hp = C / 7, r = C - hp * 7;
      const int head = 2 * hp + (r > 3 ? 1 : 0), cc = r > 3 ? r - 3 : r;
      u32x4 v = {s0[0], s1[0], s0[1], s1[1]};
      if (r == 3) {
        const u32x4 v2 = {padv, 0u, s0[1], s1[1]};
        stg(rs, v2, base0 + (head + 1) * hstride, 0);
        v[2] = padv; v[3] = 0u;
      }
      stg(rs, v, base0 + head * hstride + (cc >> 1) * 1024 + (cc & 1) * 512, 0);
    }
  }
}

template <bool V1, bool V2>
DI void p1_sweep1(const P1Ctx& c, char* lds, bf16x8 (&xf)[28], f32x16& acc0, f32x16& acc1, int wl) {
  const int lane = c.lane, r32 = lane & 31, hh = lane >> 5;
  const int xrow = (c.wid & 3) * 32 + r32, xsw = (xrow >> 1) & 7;
  lds_cptr xb = (lds_cptr)lds + L_X + xrow * 128;
  bf16x8 wf[2][8];
#pragma unroll
  for (int ch = 0; ch < 8; ++ch) {
    if (ch == 7) __builtin_amdgcn_sched_barrier(0);
    else if (ch == 0) WAITBAR(8);
    else if (ch <= 4) WAITBAR(6);
    else if (ch == 5) WAITBAR(4);
    else if (ch == 6) WAITBAR(4);
    if (ch == 0) STAMP(9);
    if (ch == 6) STAMP(11);
    lds_cptr wb = (lds_cptr)lds + L_WR + (ch % 3) * 16384 + wl;
    lds_cptr xc = xb + (ch == 6 ? 0 : ch) * 16384;
#pragma unroll
    for (int s = 0; s < 4; ++s) {
      if (ch < 7) {
        xf[ch * 4 + s] = ldsrd16(xc + (((2 * s + hh) ^ xsw) * 16));
        lds_cptr wp = wb + s * 2048;
        wf[ch & 1][s] = cat8(trrd(wp), trrd(wp + 512));
        wf[ch & 1][4 + s] = cat8(trrd(wp + 8192), trrd(wp + 8192 + 512));
      }
      if (ch >= 1) {
        const bf16x8 x8 = xf[(ch - 1) * 4 + s];
        const bf16x8 w0 = wf[(ch - 1) & 1][s], w1 = wf[(ch - 1) & 1][4 + s];
        acc0 = V1 ? MFMA(x8, w0, acc0) : MFMA(w0, x8, acc0);
        acc1 = V2 ? MFMA(x8, w1, acc1) : MFMA(w1, x8, acc1);
      }
      __builtin_amdgcn_sched_barrier(0);
      if (s == 0 && ch >= 1 && ch <= 4) p1_w12(c, ch + 2);
      if (s == 1 && ch <= 3) p1_x(c, ch + 3);
      if (s == 0 && ch == 5) { p1_w3(c, 0); p1_w3(c, 1); }
      if (s == 1 && ch == 5) { p1_w3(c, 2); p1_w3(c, 3); }
      if (s == 0 && ch == 6) { p1_w3(c, 4); p1_w3(c, 5); }
      if (s == 1 && ch == 6) { p1_w3(c, 6); }
      __builtin_amdgcn_sched_barrier(0);
    }
  }
}
template <bool V3, int C0, int C1>
DI void p1_sweep2(char* lds, const bf16x8 (&xf)[28], f32x16& acc0, int wl) {
#pragma unroll
  for (int ch = C0; ch < C1; ++ch) {
    lds_cptr wb = (lds_cptr)lds + w3_off(ch) + wl;
#pragma unroll
    for (int s = 0; s < 4; ++s) {
      lds_cptr wp = wb + s * 2048;
      const bf16x8 w0 = cat8(trrd(wp), trrd(wp + 512));
      acc0 = V3 ? MFMA(xf[ch * 4 + s], w0, acc0) : MFMA(w0, xf[ch * 4 + s], acc0);
    }
  }
}

__global__ __launch_bounds__(512, 1) void fused_k(const u16* __restrict__ x, const u16* __restrict__ wA,
                                                  const u16* __restrict__ wB, const u16* __restrict__ woA,
                                                  const u16* __restrict__ woB, const u16* __restrict__ gw,
                                                  char* ws, u16* __restrict__ out) {
  extern __shared__ __attribute__((aligned(16))) char lds[];
  const int tid = threadIdx.x, lane = tid & 63;
  const int wid = __builtin_amdgcn_readfirstlane(tid >> 6);
  const int b = blockIdx.x;
  const int r32 = lane & 31, hh = lane >> 5;
  const int g16 = lane >> 4, i16 = lane & 15, q4 = i16 >> 2, p4 = i16 & 3, cb = g16 & 1;
  const unsigned lds0 = (unsigned)(uintptr_t)lds;
  const __amdgpu_buffer_rsrc_t rs = __builtin_amdgcn_make_buffer_rsrc((void*)ws, 0, (int)WS_END, 0x00020000);
  const u64 tag = ((gm_dispatch_id() + 1) * 0x9E3779B97F4A7C15ull) ^ (u64)(uintptr_t)__builtin_amdgcn_dispatch_ptr() ^ 0x5A17C3E9D2B40F61ull;
  const f32x16 zero = {};
  const unsigned l16 = lane * 16;
  STAMP(0);
  const int h = (b & 7) + 8 * ((b >> 3) & 1), rb = b >> 4;
  unsigned fb_off = 0;
  f32x16 dA0 = zero, dA1 = zero;
  int d_w3 = 0, d_w2 = 0, d_n = 0, d_t64 = 0;
  if (tid < 8) ((__attribute__((address_space(3))) unsigned*)((lds_ptr)lds + L_CNT))[tid] = 0u;

  if (b < 224) {
    const int i5 = b >> 3, g = 7 * (b & 1) + i5 % 7, rb1 = 4 * ((b & 7) >> 1) + i5 / 7;
    const int w1 = g < 7 ? 2 * g : 7 + g, w2 = g < 7 ? 2 * g + 1 : 2 * g - 7, w3 = g < 7 ? g : 2 * g - 6;
    const int br2 = g < 7 ? 0 : 1;
    P1Ctx c;
    c.x = (const char*)x; c.lds0 = lds0; c.wid = wid; c.lane = lane;
    c.rot = (i5 % 7 + 2 * (i5 / 7)) % 7;
    c.w1 = (const char*)wB + w1 * 128;
    c.w2 = (const char*)(g < 7 ? wB : wA) + w2 * 128;
    c.w3 = (const char*)wA + w3 * 128;
    {
      const int rl8 = lane >> 3, row0 = 8 * wid + rl8, row1 = row0 + 64;
      c.xoff0 = (unsigned)((rb1 * 128 + row0) * 896 + (((lane & 7) ^ ((row0 >> 1) & 7)) * 16));
      c.xoff1 = (unsigned)((rb1 * 128 + row1) * 896 + (((lane & 7) ^ ((row1 >> 1) & 7)) * 16));
      c.woff = (unsigned)(row0 * 2688 + (((lane & 7) ^ (((rl8 >> 1) & 1) << 2)) * 16));
    }
    if (wid == 7 && lane < 56) glds16(gw, lane * 16, lds0 + L_GW);
    p1_x(c, 0); p1_w12(c, 0);
    asm volatile("s_barrier" ::: "memory");
    p1_x(c, 1); p1_w12(c, 1); p1_x(c, 2); p1_w12(c, 2);
    const int rt = wid & 3, ct = wid >> 2, t64 = rb1 * 4 + rt;
    const int wl = (8 * hh + q4) * 128 + ((ct ^ (q4 >> 1)) * 64 + cb * 32 + p4 * 8);
    bf16x8 xf[28];
    f32x16 acc0 = zero, acc1 = zero;
    if (g < 7) p1_sweep1<false, false>(c, lds, xf, acc0, acc1, wl);
    else if (g < 11) p1_sweep1<true, false>(c, lds, xf, acc0, acc1, wl);
    else p1_sweep1<true, true>(c, lds, xf, acc0, acc1, wl);
    if (g < 7) {
      f32x16 cur = acc0;
#pragma unroll 1
      for (int it = 0; it < 2; ++it) { p1_store<false>(cur, 0, w1 + it, ct, t64, lane, rs); cur = acc1; }
    } else {
      p1_store<true>(acc0, 0, w1, ct, t64, lane, rs);
    }
    STAMP(1);
    WAITBAR(4);
    acc0 = zero;
    if (g < 10) p1_sweep2<false, 0, 4>(lds, xf, acc0, wl); else p1_sweep2<true, 0, 4>(lds, xf, acc0, wl);
    asm volatile("s_waitcnt vmcnt(0)" ::: "memory");
    if (lane == 0) {
      const unsigned old = __hip_atomic_fetch_add((__attribute__((address_space(3))) unsigned*)((lds_ptr)lds + L_CNT + 4), 1u, __ATOMIC_RELAXED, __HIP_MEMORY_SCOPE_WORKGROUP);
      if (old == 7) { set_flag(ws, WS_FA + (g * 16 + rb1) * 128, tag); STAMPX(13); }
    }
    if (g < 10) p1_sweep2<false, 4, 7>(lds, xf, acc0, wl); else p1_sweep2<true, 4, 7>(lds, xf, acc0, wl);
    if (wid == g + 4) {
      float z = 0.f;
      lds_cptr gp = (lds_cptr)lds + L_GW + hh * 16;
#pragma unroll
      for (int k = 0; k < 28; ++k) {
        const u32x4 gv = __builtin_bit_cast(u32x4, ldsrd16(gp + rotc(c, k >> 2) * 128 + (k & 3) * 32));
        const u32x4 xv = __builtin_bit_cast(u32x4, xf[k]);
#pragma unroll
        for (int e = 0; e < 4; ++e) {
          const unsigned xe = xv[e], ge = gv[e];
          z = __builtin_amdgcn_fdot2_f32_bf16(__builtin_bit_cast(bf2_t, xe), __builtin_bit_cast(bf2_t, ge), z, false);
        }
      }
      z = xhalf_sum(z);
      const float gt = 1.0f / (1.0f + __expf(-z));
      if (hh == 0) __builtin_amdgcn_raw_buffer_store_b32(__float_as_uint(gt), rs, r32 * 4, WS_G + t64 * 128, 16);
    }
    dA0 = acc0; dA1 = acc1; d_w3 = w3; d_w2 = w2; d_n = 1 + br2; d_t64 = t64;
    fb_off = WS_FB + (g * 16 + rb1) * 128;
  } else {
    const int q = (b - 224) >> 3;
    unsigned acc = 0;
    const char* pc = (const char*)(__builtin_amdgcn_s_getpc() & ~127ull);
    if (q == 0 && tid < CODE_LINES) acc ^= *(const volatile unsigned*)(pc + tid * 128);
#pragma unroll
    for (int i = 0; i < 3; ++i) {
      const int line = (q * 3 + i) * 512 + tid;
      if (line < 3136) acc ^= *(const volatile unsigned*)((const char*)woB + line * 128);
      else if (line < 6272) acc ^= *(const volatile unsigned*)((const char*)woA + (line - 3136) * 128);
    }
    if (q == 3 && tid < 128) acc ^= *(const volatile unsigned*)((const char*)woA + (6144 - 3136 + tid) * 128);
    asm volatile("" :: "v"(acc));
  }

  BARRIER();
  STAMP(3);
  poll_fa(ws, lane, wid, h, rb, tag);
  STAMP(4);
  f32x16 o0 = zero, o1 = zero, o2 = zero, o3 = zero;
  {
    const unsigned kvh = WS_KV + (h * 64) * 4096;
    const unsigned qb = WS_QF + (h * 64 + rb * 4) * 2048;
    bf16x8 q00 = ldg(rs, l16, qb), q01 = ldg(rs, l16 + 1024, qb);
    bf16x8 q10 = ldg(rs, l16 + 2048, qb), q11 = ldg(rs, l16 + 3072, qb);
    bf16x8 q20 = ldg(rs, l16, qb + 4096), q21 = ldg(rs, l16 + 1024, qb + 4096);
    bf16x8 q30 = ldg(rs, l16 + 2048, qb + 4096), q31 = ldg(rs, l16 + 3072, qb + 4096);
    const bf16x8 kr0 = ldg(rs, l16, kvh), kr1 = ldg(rs, l16 + 1024, kvh);
    const unsigned kvw = kvh + wid * 8 * 4096;
#define TADDR(t) (kvw + (unsigned)((((t) + rb) & 7) * 4096))
    bf16x8 ka0 = ldg(rs, l16, TADDR(0)), ka1 = ldg(rs, l16 + 1024, TADDR(0));
    bf16x8 kb0 = ldg(rs, l16, TADDR(1)), kb1 = ldg(rs, l16 + 1024, TADDR(1));
    bf16x8 va0 = ldg(rs, l16 + 2048, TADDR(0)), va1 = ldg(rs, l16 + 3072, TADDR(0));
    bf16x8 kc0 = ldg(rs, l16, TADDR(2)), kc1 = ldg(rs, l16 + 1024, TADDR(2));
    bf16x8 vb0 = ldg(rs, l16 + 2048, TADDR(1)), vb1 = ldg(rs, l16 + 3072, TADDR(1));
    bf16x8 vc0, vc1;
    if (wid >= 4) __builtin_amdgcn_s_setprio(1);
    const bool odd = h & 1;
    const bool mylane = odd ? !hh : hh;
#define SETM(qa, qb_)                                                                   \
  {                                                                                     \
    f32x16 s = MFMA(kr0, qa, zero); s = MFMA(kr1, qb_, s);                              \
    const unsigned nm = pk2(-xhalf_max(max16(s)), 0.f);                                 \
    u32x4 ta = __builtin_bit_cast(u32x4, qa), tb = __builtin_bit_cast(u32x4, qb_);      \
    if (mylane) { if (odd) ta[0] = nm; else tb[2] = nm; }                               \
    qa = __builtin_bit_cast(bf16x8, ta); qb_ = __builtin_bit_cast(bf16x8, tb);          \
  }
    SETM(q00, q01) SETM(q10, q11) SETM(q20, q21) SETM(q30, q31)
#undef SETM
    f32x16 s0 = MFMA(ka0, q00, zero); s0 = MFMA(ka1, q01, s0);
    f32x16 s1 = MFMA(ka0, q10, zero); s1 = MFMA(ka1, q11, s1);
    f32x16 s2 = MFMA(ka0, q20, zero); s2 = MFMA(ka1, q21, s2);
    f32x16 s3 = zero;
    u32x4 pa, pb0, pb1, pb2, pb3;
    STAMP(14);
    {
      f32x16 cur = dA0;
      int wsel = d_w3;
#pragma unroll 1
      for (int it = 0; it < d_n; ++it) {
        if (wsel >= 14) p1_store<true>(cur, 1, wsel, wid >> 2, d_t64, lane, rs); else p1_store<false>(cur, 1, wsel, wid >> 2, d_t64, lane, rs);
        cur = dA1; wsel = d_w2;
      }
    }
#define SB() __builtin_amdgcn_sched_barrier(0)
#define PIN(x) asm volatile("" : "+v"(x))
#define EX4(S, i) { S[i] = __builtin_amdgcn_exp2f(S[i]); S[i + 1] = __builtin_amdgcn_exp2f(S[i + 1]); S[i + 2] = __builtin_amdgcn_exp2f(S[i + 2]); S[i + 3] = __builtin_amdgcn_exp2f(S[i + 3]); }
#define BLK(S, O, PB, V0, PS, PO, PPB, PV1, PK0, PK1, PQA, PQB, DOPV, DOQK)              \
  {                                                                                      \
    if (DOPV) PO = MFMA(PV1, __builtin_bit_cast(bf16x8, PPB), PO);                       \
    EX4(S, 0) PIN(S); SB();                                                              \
    if (DOQK) PS = MFMA(PK0, PQA, zero);                                                 \
    EX4(S, 4) pa = pack8u(S, 0); PIN(S); PIN(pa); SB();                                  \
    if (DOQK) PS = MFMA(PK1, PQB, PS);                                                   \
    EX4(S, 8) PIN(S); SB();                                                              \
    O = MFMA(V0, __builtin_bit_cast(bf16x8, pa), O);                                     \
    EX4(S, 12) PB = pack8u(S, 8); PIN(PB); SB();                                         \
  }
#define STEP(t, V0, V1, K0, K1, PV0, PV1, PK0, PK1)                                      \
  {                                                                                      \
    BLK(s0, o0, pb0, V0, s3, o3, pb3, PV1, PK0, PK1, q30, q31, t > 0, true)              \
    if (t == 1 && fb_off) {   \
      asm volatile("s_waitcnt vmcnt(4)" ::: "memory");                                   \
      if (lane == 0) {                                                                   \
        const unsigned old = __hip_atomic_fetch_add((__attribute__((address_space(3))) unsigned*)((lds_ptr)lds + L_CNT), 1u, __ATOMIC_RELAXED, __HIP_MEMORY_SCOPE_WORKGROUP); \
        if (old == 7) set_flag(ws, fb_off, tag);                                         \
      }                                                                                  \
      SB();                                                                              \
    }                                                                                    \
    if (t + 3 < 8) { PK0 = ldg(rs, l16, TADDR(t + 3)); PK1 = ldg(rs, l16 + 1024, TADDR(t + 3)); }        \
    if (t + 2 < 8) { PV0 = ldg(rs, l16 + 2048, TADDR(t + 2)); PV1 = ldg(rs, l16 + 3072, TADDR(t + 2)); } \
    SB();                                                                                \
    BLK(s1, o1, pb1, V0, s0, o0, pb0, V1, K0, K1, q00, q01, true, t < 7)                 \
    BLK(s2, o2, pb2, V0, s1, o1, pb1, V1, K0, K1, q10, q11, true, t < 7)                 \
    BLK(s3, o3, pb3, V0, s2, o2, pb2, V1, K0, K1, q20, q21, true, t < 7)                 \
  }
    STEP(0, va0, va1, kb0, kb1, vc0, vc1, ka0, ka1)
    STEP(1, vb0, vb1, kc0, kc1, va0, va1, kb0, kb1)
    STEP(2, vc0, vc1, ka0, ka1, vb0, vb1, kc0, kc1)
    STEP(3, va0, va1, kb0, kb1, vc0, vc1, ka0, ka1)
    STEP(4, vb0, vb1, kc0, kc1, va0, va1, kb0, kb1)
    STEP(5, vc0, vc1, ka0, ka1, vb0, vb1, kc0, kc1)
    STEP(6, va0, va1, kb0, kb1, vc0, vc1, ka0, ka1)
    STEP(7, vb0, vb1, kc0, kc1, va0, va1, kb0, kb1)
    o3 = MFMA(vb1, __builtin_bit_cast(bf16x8, pb3), o3);
#undef TADDR
#undef BLK
#undef EX4
#undef STEP
  }
  STAMP(5);
  STAMPW(12, 4);
  typedef __attribute__((address_space(3))) f32x4* lf4;
  const int i5 = b >> 3, rg = (b & 7) + 8 * (i5 / 7), cg3 = i5 % 7;
  const unsigned wo_vo = (unsigned)(((wid & 3) * 8 + (lane >> 3)) * 896 + cg3 * 128 + (((lane & 7) ^ (((lane >> 4) & 1) << 2)) * 16));
#define WRT(dst, O)                                                                     \
  {                                                                                     \
    lds_ptr p_ = (lds_ptr)lds + L_CB + (dst) * 16384 + (wid & 3) * 4096 + l16;          \
    *(lf4)p_ = (f32x4){O[0], O[1], O[2], O[3]};                                         \
    *(lf4)(p_ + 1024) = (f32x4){O[4], O[5], O[6], O[7]};                                \
    *(lf4)(p_ + 2048) = (f32x4){O[8], O[9], O[10], O[11]};                              \
    *(lf4)(p_ + 3072) = (f32x4){O[12], O[13], O[14], O[15]};                            \
  }
#define RDADD(ACC)                                                                      \
  {                                                                                     \
    lds_ptr p_ = (lds_ptr)lds + L_CB + (wid & 3) * 16384 + l16;                         \
    _Pragma("unroll") for (int src = 0; src < 4; ++src)                                 \
      _Pragma("unroll") for (int q_ = 0; q_ < 4; ++q_) {                                \
        const f32x4 t_ = *(lf4)(p_ + src * 4096 + q_ * 1024);                           \
        ACC[4 * q_] += t_[0]; ACC[4 * q_ + 1] += t_[1]; ACC[4 * q_ + 2] += t_[2]; ACC[4 * q_ + 3] += t_[3]; \
      }                                                                                 \
  }
  f32x16 oh = zero;
  if (wid >= 4) {
    WRT(0, o0) WRT(1, o1) WRT(2, o2) WRT(3, o3)
    asm volatile("s_waitcnt lgkmcnt(0)" ::: "memory");
    if (lane == 0) lds_arrive(lds, 3);
    {
      const int pr = rb > 0 ? rb - 1 : rb;
      const int cw = (lane & 1) ? (28 * h + 27) >> 6 : (28 * h) >> 6;
      const int kind = lane >> 1;
      const int gg = kind == 0 ? cw : (kind & 1) ? 7 + (cw >> 1) : 7 + ((7 + cw) >> 1);
      wait_flags(ws, WS_FB + (gg * 16 + (kind >= 3 ? pr : rb)) * 128, lane < 10, tag);
    }
    const int t64 = rb * 4 + wid - 4;
    const unsigned qo = WS_QF + ((16 + h) * 64 + t64) * 2048;
    const unsigned kc = WS_KV + ((16 + h) * 64 + t64) * 4096;
    const unsigned kp = t64 > 0 ? kc - 4096 : kc;
    const bf16x8 q0 = ldg(rs, l16, qo), q1 = ldg(rs, l16 + 1024, qo);
    const bf16x8 kc0 = ldg(rs, l16, kc), kc1 = ldg(rs, l16 + 1024, kc), vc0 = ldg(rs, l16 + 2048, kc), vc1 = ldg(rs, l16 + 3072, kc);
    const bf16x8 kp0 = ldg(rs, l16, kp), kp1 = ldg(rs, l16 + 1024, kp), vp0 = ldg(rs, l16 + 2048, kp), vp1 = ldg(rs, l16 + 3072, kp);
    lds_wait(lds, 3, 4);
    RDADD(oh)
    asm volatile("s_waitcnt lgkmcnt(0)" ::: "memory");
    if (lane == 0) lds_arrive(lds, 4);
    f32x16 sc = MFMA(kc0, q0, zero); sc = MFMA(kc1, q1, sc);
    f32x16 sp = MFMA(kp0, q0, zero); sp = MFMA(kp1, q1, sp);
#pragma unroll
    for (int r = 0; r < 16; ++r) {
      const int i = (r & 3) + 8 * (r >> 2) + 4 * hh;
      const int dc = r32 - i, dp = r32 + 32 - i;
      const bool vc = (dc >= 0) && (dc <= 14) && !(dc & 1);
      const bool vp = (dp <= 14) && !(dp & 1) && (t64 > 0);
      sc[r] = vc ? sc[r] : -INFINITY;
      sp[r] = vp ? sp[r] : -INFINITY;
    }
    const float m = xhalf_max(fmaxf(max16(sc), max16(sp)));
#pragma unroll
    for (int r = 0; r < 16; ++r) {
      sc[r] = __builtin_amdgcn_exp2f(sc[r] - m);
      sp[r] = __builtin_amdgcn_exp2f(sp[r] - m);
    }
    f32x16 o = MFMA(vc0, pack8(sc, 0), zero);
    o = MFMA(vc1, pack8(sc, 8), o);
    o = MFMA(vp0, pack8(sp, 0), o);
    o = MFMA(vp1, pack8(sp, 8), o);
    const float l = __shfl(o[12], 32 + r32);
    const float rl = 1.0f / l;
#pragma unroll
    for (int r = 0; r < 16; ++r) o[r] *= rl;
    if (hh) { o[12] = 0.f; o[13] = 0.f; o[14] = 0.f; o[15] = 0.f; }
    const unsigned od = WS_OF + ((64 + t64) * 16 + h) * 2048;
    stg(rs, pack8u(o, 0), l16, od);
    stg(rs, pack8u(o, 8), l16 + 1024, od);
    if (b < 224) {
#pragma unroll
      for (int i = 0; i < 8; ++i) glds16((const char*)woA + i * 32 * 896, wo_vo, lds0 + L_WOA + (i * 4 + wid - 4) * 1024);
      asm volatile("s_waitcnt vmcnt(8)" ::: "memory");
    } else {
      asm volatile("s_waitcnt vmcnt(0)" ::: "memory");
    }
    if (lane == 0) set_flag(ws, WS_F3A + (t64 * 16 + h) * 128, tag);
    if (b < 224) {
#pragma unroll
      for (int i = 8; i < 14; ++i) glds16((const char*)woA + i * 32 * 896, wo_vo, lds0 + L_WOA + (i * 4 + wid - 4) * 1024);
#pragma unroll
      for (int i = 0; i < 7; ++i) glds16((const char*)woB + i * 32 * 896, wo_vo, lds0 + L_WOB1 + (i * 4 + wid - 4) * 1024);
      if (wid == 4) glds16((const char*)woB + 7 * 32 * 896, wo_vo, lds0 + L_WOB1 + 28 * 1024);
      if (wid == 5 && lane < 32) *(__attribute__((address_space(3))) u32x4*)((lds_ptr)lds + L_WOA + 448 * 128 + lane * 16) = (u32x4){0u, 0u, 0u, 0u};
    }
    STAMPW(2, 4);
  } else {
    lds_wait(lds, 4, 4);
    WRT(0, o0) WRT(1, o1) WRT(2, o2) WRT(3, o3)
  }
  BARRIER();
  STAMP(15);
  if (wid >= 4) {
    RDADD(oh)
    float l = __shfl(oh[12], 32 + r32);
    const int t64 = rb * 4 + wid - 4;
    if (__any(!(l < 1.0e18f))) {
      const unsigned qo = WS_QF + (h * 64 + t64) * 2048;
      const bf16x8 q0 = ldg(rs, l16, qo), q1 = ldg(rs, l16 + 1024, qo);
      float m = -INFINITY;
      oh = zero;
#pragma unroll 1
      for (int t = 0; t < 64; ++t) {
        const unsigned kv = WS_KV + (h * 64 + t) * 4096;
        const bf16x8 k0 = ldg(rs, l16, kv), k1 = ldg(rs, l16 + 1024, kv), v0 = ldg(rs, l16 + 2048, kv), v1 = ldg(rs, l16 + 3072, kv);
        f32x16 sx = MFMA(k0, q0, zero); sx = MFMA(k1, q1, sx);
        const float mn = fmaxf(m, xhalf_max(max16(sx)));
        const float al = __builtin_amdgcn_exp2f(m - mn);
        m = mn;
#pragma unroll
        for (int r = 0; r < 16; ++r) { oh[r] *= al; sx[r] = __builtin_amdgcn_exp2f(sx[r] - mn); }
        oh = MFMA(v0, pack8(sx, 0), oh);
        oh = MFMA(v1, pack8(sx, 8), oh);
      }
      l = __shfl(oh[12], 32 + r32);
    }
    const float rl = 1.0f / l;
#pragma unroll
    for (int r = 0; r < 16; ++r) oh[r] *= rl;
    if (hh) { oh[12] = 0.f; oh[13] = 0.f; oh[14] = 0.f; oh[15] = 0.f; }
    const unsigned od = WS_OF + (t64 * 16 + h) * 2048;
    stg(rs, pack8u(oh, 0), l16, od);
    stg(rs, pack8u(oh, 8), l16 + 1024, od);
  }
#undef WRT
#undef RDADD
  if (wid >= 4) WAITBAR(2); else BARRIER();
  if (wid >= 4) {
    if (b >= 224) {
      asm volatile("s_waitcnt vmcnt(0)" ::: "memory");
      if (lane == 0 && lds_arrive(lds, 2) == 3) set_flag(ws, WS_F3 + (rb * 16 + h) * 128, tag);
    }
  } else if (b < 224) {
#pragma unroll
    for (int i = 0; i < 7; ++i) glds16((const char*)woB + (224 + i * 32) * 896, wo_vo, lds0 + L_CB + (i * 4 + wid) * 1024);
    if (wid == 0 && lane < 32) *(__attribute__((address_space(3))) u32x4*)((lds_ptr)lds + L_CB + 224 * 128 + lane * 16) = (u32x4){0u, 0u, 0u, 0u};
  }
  if (b >= 224) { STAMPDUMP(); return; }

  {
    const int rt = wid & 1, kq = wid >> 1;
    wait_flags(ws, lane < 4 ? WS_F3A + ((rg * 2 + rt) * 16 + kq * 4 + lane) * 128 : WS_FB + (((rg * 2 + rt) & 3) * 16 + (rg >> 1)) * 128,
               lane < 5, tag);
    if (wid >= 4) {
      asm volatile("s_waitcnt vmcnt(0)" ::: "memory");
      if (lane == 0 && lds_arrive(lds, 2) == 3) { set_flag(ws, WS_F3 + (rb * 16 + h) * 128, tag); STAMPX(6); }
    }
    const float g = __uint_as_float(__builtin_amdgcn_raw_buffer_load_b32(rs, r32 * 4, WS_G + (rg * 2 + rt) * 128, 16));
    const unsigned oa = WS_OF + ((64 + rg * 2 + rt) * 16 + kq * 4) * 2048;
    bf16x8 of[8], og[8];
#pragma unroll
    for (int i = 0; i < 8; ++i) of[i] = ldg(rs, l16 + (i & 3) * 1024, oa + (i >> 2) * 4096);
    STAMP(7);
    wait_flags(ws, WS_F3 + ((rg >> 1) * 16 + kq * 4 + lane) * 128, lane < 4, tag);
    STAMP(10);
    const unsigned ob = WS_OF + ((rg * 2 + rt) * 16 + kq * 4) * 2048;
#pragma unroll
    for (int i = 0; i < 8; ++i) og[i] = ldg(rs, l16 + (i & 3) * 1024, ob + (i >> 2) * 4096);
    const int wlo = (4 * hh + q4) * 128 + cb * 32 + p4 * 8;
    lds_cptr wpa = (lds_cptr)lds + L_WOA + kq * 4 * 28 * 128 + wlo;
    const int sw = (q4 >> 1) * 64;
    f32x16 a0 = zero, a1 = zero;
#pragma unroll
    for (int i = 0; i < 8; ++i) {
      lds_cptr p = wpa + ((i >> 1) * 28 + 16 * (i & 1)) * 128;
      const bf16x8 wf0 = cat8(trrd(p + sw), trrd(p + sw + 8 * 128));
      const bf16x8 wf1 = cat8(trrd(p + (64 - sw)), trrd(p + (64 - sw) + 8 * 128));
      a0 = MFMA(wf0, of[i], a0);
      a1 = MFMA(wf1, of[i], a1);
    }
    WAITBAR(8);
    lds_cptr wpb = (lds_cptr)lds + (kq < 2 ? L_WOB1 + kq * 4 * 28 * 128 : L_CB + (kq - 2) * 4 * 28 * 128) + wlo;
    f32x16 b0 = zero, b1 = zero;
#pragma unroll
    for (int i = 0; i < 8; ++i) {
      lds_cptr p = wpb + ((i >> 1) * 28 + 16 * (i & 1)) * 128;
      const bf16x8 wf0 = cat8(trrd(p + sw), trrd(p + sw + 8 * 128));
      const bf16x8 wf1 = cat8(trrd(p + (64 - sw)), trrd(p + (64 - sw) + 8 * 128));
      b0 = MFMA(wf0, og[i], b0);
      b1 = MFMA(wf1, og[i], b1);
    }
    const float g1 = 1.f - g;
#pragma unroll
    for (int r = 0; r < 16; ++r) { a0[r] = g * a0[r] + g1 * b0[r]; a1[r] = g * a1[r] + g1 * b1[r]; }
    BARRIER();
    lds_ptr ab = (lds_ptr)lds + L_WOA + rt * 32768 + kq * 2048 + l16;
#define WRQ(dst, A, o8)                                                                 \
  {                                                                                     \
    lds_ptr p = ab + (dst) * 8192;                                                      \
    *(lf4)p = (f32x4){A[o8], A[o8 + 1], A[o8 + 2], A[o8 + 3]};                          \
    *(lf4)(p + 1024) = (f32x4){A[o8 + 4], A[o8 + 5], A[o8 + 6], A[o8 + 7]};             \
  }
    WRQ(0, a0, 0) WRQ(1, a0, 8) WRQ(2, a1, 0) WRQ(3, a1, 8)
#undef WRQ
    BARRIER();
    {
      lds_ptr p = (lds_ptr)lds + L_WOA + rt * 32768 + kq * 8192 + l16;
      f32x4 u = *(lf4)p, w = *(lf4)(p + 1024);
#pragma unroll
      for (int src = 1; src < 4; ++src) { u += *(lf4)(p + src * 2048); w += *(lf4)(p + src * 2048 + 1024); }
      const int row = rg * 64 + rt * 32 + r32;
      const int col = cg3 * 64 + (kq >> 1) * 32 + (kq & 1) * 16 + 4 * hh;
      *(u32x2*)(out + (size_t)row * 448 + col) = (u32x2){pk2(u[0], u[1]), pk2(u[2], u[3])};
      *(u32x2*)(out + (size_t)row * 448 + col + 8) = (u32x2){pk2(w[0], w[1]), pk2(w[2], w[3])};
    }
  }
  STAMP(8);
  STAMPDUMP();
}

extern "C" void kernel_launch(void* const* d_in, const int* in_sizes, int n_in, void* d_out, int out_size,
                              void* d_ws, size_t ws_size, hipStream_t stream) {
  const u16* x = (const u16*)d_in[0];
  const u16* wqkvA = (const u16*)d_in[1];
  const u16* woA = (const u16*)d_in[2];
  const u16* wqkvB = (const u16*)d_in[4];
  const u16* woB = (const u16*)d_in[5];
  const u16* gw = (const u16*)d_in[6];
  (void)hipFuncSetAttribute((const void*)fused_k, hipFuncAttributeMaxDynamicSharedMemorySize, LDS_BYTES);
  hipLaunchKernelGGL(fused_k, dim3(256), dim3(512), LDS_BYTES, stream, x, wqkvA, wqkvB, woA, woB, gw, (char*)d_ws, (u16*)d_out);
}
```

```cpp
#include <hip/hip_runtime.h>
#include <hip/hip_bf16.h>
#include <stdint.h>

typedef unsigned short u16;
typedef unsigned long long u64;
typedef short bf16x8 __attribute__((ext_vector_type(8)));
typedef short s16x4 __attribute__((ext_vector_type(4)));
typedef float f32x16 __attribute__((ext_vector_type(16)));
typedef float f32x2 __attribute__((ext_vector_type(2)));
typedef float f32x4 __attribute__((ext_vector_type(4)));
typedef unsigned u32x4 __attribute__((ext_vector_type(4)));
typedef unsigned u32x2 __attribute__((ext_vector_type(2)));
typedef __bf16 bf2_t __attribute__((ext_vector_type(2)));
typedef __attribute__((address_space(3))) const char* lds_cptr;
typedef __attribute__((address_space(3))) char* lds_ptr;
typedef __attribute__((address_space(1))) u64 gu64;
typedef short v4i16_t __attribute__((ext_vector_type(4)));

#define DI __device__ __forceinline__
#define POLL_SLEEP 10
#define STAMP(i)
#define STAMPW(i, w)
#define STAMPX(i)
#define STAMPDUMP()
#define MFMA(a, b, c) __builtin_amdgcn_mfma_f32_32x32x16_bf16((a), (b), (c), 0, 0, 0)
#define BARRIER() asm volatile("s_waitcnt lgkmcnt(0)\n\ts_barrier" ::: "memory")
#define WAITBAR(N) asm volatile("s_waitcnt vmcnt(" #N ") lgkmcnt(0)\n\ts_barrier" ::: "memory")

extern "C" __device__ u64 gm_dispatch_id() __asm("llvm.amdgcn.dispatch.id");

#define CODE_LINES 264
constexpr float QSCALE = 0.18898223650461362f * 1.4426950408889634f;

constexpr unsigned WS_QF = 0;
constexpr unsigned WS_KV = 4u << 20;
constexpr unsigned WS_OF = 12u << 20;
constexpr unsigned WS_G = 16u << 20;
constexpr unsigned WS_FA = WS_G + 65536;
constexpr unsigned WS_FB = WS_FA + 32768;
constexpr unsigned WS_F3 = WS_FB + 32768;
constexpr unsigned WS_F3A = WS_F3 + 32768;
constexpr unsigned WS_END = WS_F3A + 131072;

constexpr int LDS_BYTES = 163840;
constexpr int L_X = 0;
constexpr int L_WR = 98304;
constexpr int L_GW = 147456;
constexpr int L_CNT = 163328;
constexpr int L_CB = 0;
constexpr int L_WOA = 65536;
constexpr int L_WOB1 = 123392;

DI unsigned pk2(float a, float b) {
  f32x2 v = {a, b};
  bf2_t r = __builtin_convertvector(v, bf2_t);
  return __builtin_bit_cast(unsigned, r);
}
DI u32x4 pack8u(const f32x16& x, int o) {
  u32x4 p;
  p[0] = pk2(x[o + 0], x[o + 1]); p[1] = pk2(x[o + 2], x[o + 3]);
  p[2] = pk2(x[o + 4], x[o + 5]); p[3] = pk2(x[o + 6], x[o + 7]);
  return p;
}
DI bf16x8 pack8(const f32x16& x, int o) { return __builtin_bit_cast(bf16x8, pack8u(x, o)); }
DI void glds16(const void* sbase, unsigned voff, unsigned lds_base) {
  unsigned sv;
  asm volatile("s_mov_b32 %0, m0\n\ts_mov_b32 m0, %3\n\ts_nop 4\n\tglobal_load_lds_dwordx4 %1, %2\n\ts_mov_b32 m0, %0"
               : "=&s"(sv) : "v"(voff), "s"(sbase), "s"(lds_base) : "memory");
}
DI s16x4 trrd(lds_cptr p) {
  return __builtin_bit_cast(s16x4, __builtin_amdgcn_ds_read_tr16_b64_v4i16((__attribute__((address_space(3))) v4i16_t*)p));
}
DI bf16x8 cat8(s16x4 lo, s16x4 hi) { return __builtin_shufflevector(lo, hi, 0, 1, 2, 3, 4, 5, 6, 7); }
DI bf16x8 ldsrd16(lds_cptr p) { return *(const __attribute__((address_space(3))) bf16x8*)p; }
DI float xhalf_max(float m) {
  auto rr = __builtin_amdgcn_permlane32_swap(__float_as_uint(m), __float_as_uint(m), false, false);
  return fmaxf(__uint_as_float(rr[0]), __uint_as_float(rr[1]));
}
DI float xhalf_sum(float m) {
  auto rr = __builtin_amdgcn_permlane32_swap(__float_as_uint(m), __float_as_uint(m), false, false);
  return __uint_as_float(rr[0]) + __uint_as_float(rr[1]);
}
DI float max16(const f32x16& s) {
  float a = fmaxf(fmaxf(s[0], s[1]), s[2]), b = fmaxf(fmaxf(s[3], s[4]), s[5]);
  a = fmaxf(fmaxf(a, s[6]), s[7]); b = fmaxf(fmaxf(b, s[8]), s[9]);
  a = fmaxf(fmaxf(a, s[10]), s[11]); b = fmaxf(fmaxf(b, s[12]), s[13]);
  a = fmaxf(fmaxf(a, s[14]), s[15]);
  return fmaxf(a, b);
}
DI bf16x8 ldg(__amdgpu_buffer_rsrc_t rs, unsigned voff, unsigned soff) {
  return __builtin_bit_cast(bf16x8, __builtin_amdgcn_raw_buffer_load_b128(rs, voff, soff, 16));
}
DI void stg(__amdgpu_buffer_rsrc_t rs, u32x4 v, unsigned voff, unsigned soff) {
  __builtin_amdgcn_raw_buffer_store_b128(v, rs, voff, soff, 16);
}
DI void wait_flags(const char* ws, unsigned off, bool active, u64 tag) {
  gu64* p = (gu64*)(ws + off);
  const u64 t0 = __builtin_amdgcn_s_memrealtime();
  for (;;) {
    u64 v = tag;
    if (active) v = __hip_atomic_load(p, __ATOMIC_RELAXED, __HIP_MEMORY_SCOPE_AGENT);
    if (__all(v == tag)) break;
    __builtin_amdgcn_s_sleep(1);
    if (__builtin_amdgcn_s_memrealtime() - t0 > 5000000ull) break;
  }
  __builtin_amdgcn_fence(__ATOMIC_ACQUIRE, "wavefront");
}
DI void set_flag(char* ws, unsigned off, u64 tag) {
  __hip_atomic_store((gu64*)(ws + off), tag, __ATOMIC_RELAXED, __HIP_MEMORY_SCOPE_AGENT);
}

DI void poll_fa(const char* ws, int lane, int wid, int h, int rb, u64 tag) {
  const int cw = (lane & 1) ? (28 * h + 27) >> 6 : (28 * h) >> 6;
  const int kind = lane >> 1;
  const int g = kind == 5 ? cw >> 1 : (kind == 2 || kind == 3) ? 7 + cw : (7 + cw) >> 1;
  const int r = kind == 5 ? rb : kind == 4 ? 0 : 2 * wid + (kind & 1);
  wait_flags(ws, WS_FA + (g * 16 + r) * 128, lane < 12, tag);
}

typedef __attribute__((address_space(3))) unsigned* lds_u32p;
DI unsigned lds_arrive(char* lds, int idx) {
  return __hip_atomic_fetch_add((lds_u32p)((lds_ptr)lds + L_CNT) + idx, 1u, __ATOMIC_RELAXED, __HIP_MEMORY_SCOPE_WORKGROUP);
}
DI void lds_wait(char* lds, int idx, unsigned n) {
  while (__hip_atomic_load((lds_u32p)((lds_ptr)lds + L_CNT) + idx, __ATOMIC_RELAXED, __HIP_MEMORY_SCOPE_WORKGROUP) < n) __builtin_amdgcn_s_sleep(1);
  asm volatile("" ::: "memory");
}

struct P1Ctx {
  const char* x; const char* w1; const char* w2; const char* w3;
  unsigned lds0, xoff0, xoff1, woff;
  int wid, lane, rot;
};
DI int rotc(const P1Ctx& c, int ch) { const int t = ch + c.rot; return t >= 7 ? t - 7 : t; }
DI void p1_x(const P1Ctx& c, int ch) {
  const int slot = ch == 6 ? 0 : ch;
  const char* src = c.x + rotc(c, ch) * 128;
  glds16(src, c.xoff0, c.lds0 + L_X + slot * 16384 + c.wid * 1024);
  glds16(src, c.xoff1, c.lds0 + L_X + slot * 16384 + (c.wid + 8) * 1024);
}
DI void p1_w12(const P1Ctx& c, int ch) {
  const int slot = ch % 3;
  const int ro = rotc(c, ch) * (64 * 2688);
  glds16(c.w1 + ro, c.woff, c.lds0 + L_WR + slot * 16384 + c.wid * 1024);
  glds16(c.w2 + ro, c.woff, c.lds0 + L_WR + slot * 16384 + 8192 + c.wid * 1024);
}
DI constexpr int w3_off(int ch) { return L_X + (1 + (ch >> 1)) * 16384 + (ch & 1) * 8192; }
DI void p1_w3(const P1Ctx& c, int ch) {
  glds16(c.w3 + rotc(c, ch) * (64 * 2688), c.woff, c.lds0 + w3_off(ch) + c.wid * 1024);
}

template <bool ISV>
DI void p1_store(const f32x16& a, int br, int w, int ct, int t64, int lane, __amdgpu_buffer_rsrc_t rs) {
  const int r32 = lane & 31, hh = lane >> 5;
  const int sec = w / 7, wc = (w - sec * 7) * 64;
  if (ISV) {
    const int c = wc + ct * 32 + r32, head = c / 28, d = c - head * 28;
    const unsigned off = WS_KV + ((br * 16 + head) * 64 + t64) * 4096 + 2048 + (hh * 32 + d) * 16;
    stg(rs, pack8u(a, 0), off, 0);
    stg(rs, pack8u(a, 8), off + 1024, 0);
    if (d >= 24) {
      const unsigned f = d == 24 ? 0x3F803F80u : 0u;
      const u32x4 pv = {f, f, f, f};
      stg(rs, pv, off + 64, 0);
      stg(rs, pv, off + 64 + 1024, 0);
    }
  } else {
    const float sc = sec == 0 ? QSCALE : 1.f;
    unsigned w8[8];
#pragma unroll
    for (int i = 0; i < 8; ++i) w8[i] = pk2(a[2 * i] * sc, a[2 * i + 1] * sc);
    const unsigned padv = sec == 0 ? 0u : 0x3F80u;
    const unsigned hstride = sec == 0 ? 64u * 2048u : 64u * 4096u;
    const unsigned base0 = (sec == 0 ? WS_QF + (br * 16 * 64 + t64) * 2048 : WS_KV + (br * 16 * 64 + t64) * 4096) + r32 * 16;
#pragma unroll
    for (int pp = 0; pp < 2; ++pp) {
      const auto s0 = __builtin_amdgcn_permlane32_swap(w8[4 * pp], w8[4 * pp + 2], false, false);
      const auto s1 = __builtin_amdgcn_permlane32_swap(w8[4 * pp + 1], w8[4 * pp + 3], false, false);
      const int C = (wc >> 3) + ct * 4 + 2 * pp + hh, hp = C / 7, r = C - hp * 7;
      const int head = 2 * hp + (r > 3 ? 1 : 0), cc = r > 3 ? r - 3 : r;
      u32x4 v = {s0[0], s1[0], s0[1], s1[1]};
      if (r == 3) {
        const u32x4 v2 = {padv, 0u, s0[1], s1[1]};
        stg(rs, v2, base0 + (head + 1) * hstride, 0);
        v[2] = padv; v[3] = 0u;
      }
      stg(rs, v, base0 + head * hstride + (cc >> 1) * 1024 + (cc & 1) * 512, 0);
    }
  }
}

template <bool V1, bool V2>
DI void p1_sweep1(const P1Ctx& c, char* lds, bf16x8 (&xf)[28], f32x16& acc0, f32x16& acc1, int wl) {
  const int lane = c.lane, r32 = lane & 31, hh = lane >> 5;
  const int xrow = (c.wid & 3) * 32 + r32, xsw = (xrow >> 1) & 7;
  lds_cptr xb = (lds_cptr)lds + L_X + xrow * 128;
  bf16x8 wf[2][8];
#pragma unroll
  for (int ch = 0; ch < 8; ++ch) {
    if (ch == 7) __builtin_amdgcn_sched_barrier(0);
    else if (ch == 0) WAITBAR(8);
    else if (ch <= 4) WAITBAR(6);
    else if (ch == 5) WAITBAR(4);
    else if (ch == 6) WAITBAR(4);
    if (ch == 0) STAMP(9);
    if (ch == 6) STAMP(11);
    lds_cptr wb = (lds_cptr)lds + L_WR + (ch % 3) * 16384 + wl;
    lds_cptr xc = xb + (ch == 6 ? 0 : ch) * 16384;
#pragma unroll
    for (int s = 0; s < 4; ++s) {
      if (ch < 7) {
        xf[ch * 4 + s] = ldsrd16(xc + (((2 * s + hh) ^ xsw) * 16));
        lds_cptr wp = wb + s * 2048;
        wf[ch & 1][s] = cat8(trrd(wp), trrd(wp + 512));
        wf[ch & 1][4 + s] = cat8(trrd(wp + 8192), trrd(wp + 8192 + 512));
      }
      if (ch >= 1) {
        const bf16x8 x8 = xf[(ch - 1) * 4 + s];
        const bf16x8 w0 = wf[(ch - 1) & 1][s], w1 = wf[(ch - 1) & 1][4 + s];
        acc0 = V1 ? MFMA(x8, w0, acc0) : MFMA(w0, x8, acc0);
        acc1 = V2 ? MFMA(x8, w1, acc1) : MFMA(w1, x8, acc1);
      }
      __builtin_amdgcn_sched_barrier(0);
      if (s == 0 && ch >= 1 && ch <= 4) p1_w12(c, ch + 2);
      if (s == 1 && ch <= 3) p1_x(c, ch + 3);
      if (s == 0 && ch == 5) { p1_w3(c, 0); p1_w3(c, 1); }
      if (s == 1 && ch == 5) { p1_w3(c, 2); p1_w3(c, 3); }
      if (s == 0 && ch == 6) { p1_w3(c, 4); p1_w3(c, 5); }
      if (s == 1 && ch == 6) { p1_w3(c, 6); }
      __builtin_amdgcn_sched_barrier(0);
    }
  }
}
template <bool V3, int C0, int C1>
DI void p1_sweep2(char* lds, const bf16x8 (&xf)[28], f32x16& acc0, int wl) {
#pragma unroll
  for (int ch = C0; ch < C1; ++ch) {
    lds_cptr wb = (lds_cptr)lds + w3_off(ch) + wl;
#pragma unroll
    for (int s = 0; s < 4; ++s) {
      lds_cptr wp = wb + s * 2048;
      const bf16x8 w0 = cat8(trrd(wp), trrd(wp + 512));
      acc0 = V3 ? MFMA(xf[ch * 4 + s], w0, acc0) : MFMA(w0, xf[ch * 4 + s], acc0);
    }
  }
}

__global__ __launch_bounds__(512, 1) void fused_k(const u16* __restrict__ x, const u16* __restrict__ wA,
                                                  const u16* __restrict__ wB, const u16* __restrict__ woA,
                                                  const u16* __restrict__ woB, const u16* __restrict__ gw,
                                                  char* ws, u16* __restrict__ out) {
  extern __shared__ __attribute__((aligned(16))) char lds[];
  asm volatile("" :: "s"(x), "s"(wA), "s"(wB), "s"(woA), "s"(woB), "s"(gw), "s"(ws), "s"(out));
  const int tid = threadIdx.x, lane = tid & 63;
  const int wid = __builtin_amdgcn_readfirstlane(tid >> 6);
  const int b = blockIdx.x;
  const int r32 = lane & 31, hh = lane >> 5;
  const int g16 = lane >> 4, i16 = lane & 15, q4 = i16 >> 2, p4 = i16 & 3, cb = g16 & 1;
  const unsigned lds0 = (unsigned)(uintptr_t)lds;
  const __amdgpu_buffer_rsrc_t rs = __builtin_amdgcn_make_buffer_rsrc((void*)ws, 0, (int)WS_END, 0x00020000);
  const u64 tag = ((gm_dispatch_id() + 1) * 0x9E3779B97F4A7C15ull) ^ (u64)(uintptr_t)__builtin_amdgcn_dispatch_ptr() ^ 0x5A17C3E9D2B40F61ull;
  const f32x16 zero = {};
  const unsigned l16 = lane * 16;
  STAMP(0);
  const int h = (b & 7) + 8 * ((b >> 3) & 1), rb = b >> 4;
  unsigned fb_off = 0;
  f32x16 dA0 = zero, dA1 = zero;
  int d_w3 = 0, d_w2 = 0, d_n = 0, d_t64 = 0;
  if (tid < 8) ((__attribute__((address_space(3))) unsigned*)((lds_ptr)lds + L_CNT))[tid] = 0u;

  if (b < 224) {
    const int i5 = b >> 3, g = (b & 1) + 2 * (i5 % 7), rb1 = 4 * ((b & 7) >> 1) + i5 / 7;
    const int w1 = g < 7 ? 2 * g : 7 + g, w2 = g < 7 ? 2 * g + 1 : 2 * g - 7, w3 = g < 7 ? g : 2 * g - 6;
    const int br2 = g < 7 ? 0 : 1;
    P1Ctx c;
    c.x = (const char*)x; c.lds0 = lds0; c.wid = wid; c.lane = lane;
    c.rot = (i5 % 7 + 2 * (i5 / 7)) % 7;
    c.w1 = (const char*)wB + w1 * 128;
    c.w2 = (const char*)(g < 7 ? wB : wA) + w2 * 128;
    c.w3 = (const char*)wA + w3 * 128;
    {
      const int rl8 = lane >> 3, row0 = 8 * wid + rl8, row1 = row0 + 64;
      c.xoff0 = (unsigned)((rb1 * 128 + row0) * 896 + (((lane & 7) ^ ((row0 >> 1) & 7)) * 16));
      c.xoff1 = (unsigned)((rb1 * 128 + row1) * 896 + (((lane & 7) ^ ((row1 >> 1) & 7)) * 16));
      c.woff = (unsigned)(row0 * 2688 + (((lane & 7) ^ (((rl8 >> 1) & 1) << 2)) * 16));
    }
    if (wid == 7 && lane < 56) glds16(gw, lane * 16, lds0 + L_GW);
    p1_x(c, 0); p1_w12(c, 0);
    asm volatile("s_barrier" ::: "memory");
    p1_x(c, 1); p1_w12(c, 1); p1_x(c, 2); p1_w12(c, 2);
    const int rt = wid & 3, ct = wid >> 2, t64 = rb1 * 4 + rt;
    const int wl = (8 * hh + q4) * 128 + ((ct ^ (q4 >> 1)) * 64 + cb * 32 + p4 * 8);
    bf16x8 xf[28];
    f32x16 acc0 = zero, acc1 = zero;
    if (g < 7) p1_sweep1<false, false>(c, lds, xf, acc0, acc1, wl);
    else if (g < 11) p1_sweep1<true, false>(c, lds, xf, acc0, acc1, wl);
    else p1_sweep1<true, true>(c, lds, xf, acc0, acc1, wl);
    if (g < 7) {
      f32x16 cur = acc0;
#pragma unroll 1
      for (int it = 0; it < 2; ++it) { p1_store<false>(cur, 0, w1 + it, ct, t64, lane, rs); cur = acc1; }
    } else {
      p1_store<true>(acc0, 0, w1, ct, t64, lane, rs);
    }
    STAMP(1);
    WAITBAR(4);
    acc0 = zero;
    if (g < 10) p1_sweep2<false, 0, 4>(lds, xf, acc0, wl); else p1_sweep2<true, 0, 4>(lds, xf, acc0, wl);
    asm volatile("s_waitcnt vmcnt(0)" ::: "memory");
    if (lane == 0) {
      const unsigned old = __hip_atomic_fetch_add((__attribute__((address_space(3))) unsigned*)((lds_ptr)lds + L_CNT + 4), 1u, __ATOMIC_RELAXED, __HIP_MEMORY_SCOPE_WORKGROUP);
      if (old == 7) { set_flag(ws, WS_FA + (g * 16 + rb1) * 128, tag); STAMPX(13); }
    }
    if (g < 10) p1_sweep2<false, 4, 7>(lds, xf, acc0, wl); else p1_sweep2<true, 4, 7>(lds, xf, acc0, wl);
    if (wid == g + 4) {
      float z = 0.f;
      lds_cptr gp = (lds_cptr)lds + L_GW + hh * 16;
#pragma unroll
      for (int k = 0; k < 28; ++k) {
        const u32x4 gv = __builtin_bit_cast(u32x4, ldsrd16(gp + rotc(c, k >> 2) * 128 + (k & 3) * 32));
        const u32x4 xv = __builtin_bit_cast(u32x4, xf[k]);
#pragma unroll
        for (int e = 0; e < 4; ++e) {
          const unsigned xe = xv[e], ge = gv[e];
          z = __builtin_amdgcn_fdot2_f32_bf16(__builtin_bit_cast(bf2_t, xe), __builtin_bit_cast(bf2_t, ge), z, false);
        }
      }
      z = xhalf_sum(z);
      const float gt = 1.0f / (1.0f + __expf(-z));
      if (hh == 0) __builtin_amdgcn_raw_buffer_store_b32(__float_as_uint(gt), rs, r32 * 4, WS_G + t64 * 128, 16);
    }
    dA0 = acc0; dA1 = acc1; d_w3 = w3; d_w2 = w2; d_n = 1 + br2; d_t64 = t64;
    fb_off = WS_FB + (g * 16 + rb1) * 128;
  } else {
    typedef const __attribute__((address_space(1))) unsigned* gptr;
    const int q = (b - 224) >> 3;
    const u64 pc = __builtin_amdgcn_s_getpc() & ~127ull;
    const int cl = min(tid * 4 + q, CODE_LINES - 1);
    const int l0 = q * 784 + tid, l1 = q * 784 + min(tid + 512, 783);
    const unsigned t0 = tid < 80 ? *(gptr)(pc + (u64)cl * 128) : 0u;
    const unsigned t1 = *(gptr)((u64)woB + (u64)l0 * 128), t2 = *(gptr)((u64)woB + (u64)l1 * 128);
    const unsigned t3 = *(gptr)((u64)woA + (u64)l0 * 128), t4 = *(gptr)((u64)woA + (u64)l1 * 128);
    const unsigned acc = t0 ^ t1 ^ t2 ^ t3 ^ t4;
    asm volatile("" :: "v"(acc));
  }

  BARRIER();
  STAMP(3);
  poll_fa(ws, lane, wid, h, rb, tag);
  STAMP(4);
  typedef __attribute__((address_space(3))) f32x4* lf4;
  const int i5 = b >> 3, rg = (b & 7) + 8 * (i5 / 7), cg3 = i5 % 7;
  const unsigned wo_vo = (unsigned)(((wid & 3) * 8 + (lane >> 3)) * 896 + cg3 * 128 + (((lane & 7) ^ (((lane >> 4) & 1) << 2)) * 16));
#define WRT(dst, O)                                                                     \
  {                                                                                     \
    lds_ptr p_ = (lds_ptr)lds + L_CB + (dst) * 16384 + (wid & 3) * 4096 + l16;          \
    *(lf4)p_ = (f32x4){O[0], O[1], O[2], O[3]};                                         \
    *(lf4)(p_ + 1024) = (f32x4){O[4], O[5], O[6], O[7]};                                \
    *(lf4)(p_ + 2048) = (f32x4){O[8], O[9], O[10], O[11]};                              \
    *(lf4)(p_ + 3072) = (f32x4){O[12], O[13], O[14], O[15]};                            \
  }
  f32x16 o0 = zero, o1 = zero, o2 = zero, o3 = zero;
  {
    const unsigned kvh = WS_KV + (h * 64) * 4096;
    const unsigned qb = WS_QF + (h * 64 + rb * 4) * 2048;
    bf16x8 q00 = ldg(rs, l16, qb), q01 = ldg(rs, l16 + 1024, qb);
    bf16x8 q10 = ldg(rs, l16 + 2048, qb), q11 = ldg(rs, l16 + 3072, qb);
    bf16x8 q20 = ldg(rs, l16, qb + 4096), q21 = ldg(rs, l16 + 1024, qb + 4096);
    bf16x8 q30 = ldg(rs, l16 + 2048, qb + 4096), q31 = ldg(rs, l16 + 3072, qb + 4096);
    const bf16x8 kr0 = ldg(rs, l16, kvh), kr1 = ldg(rs, l16 + 1024, kvh);
    const unsigned kvw = kvh + wid * 8 * 4096;
#define TADDR(t) (kvw + (unsigned)((((t) + rb) & 7) * 4096))
    bf16x8 ka0 = ldg(rs, l16, TADDR(0)), ka1 = ldg(rs, l16 + 1024, TADDR(0));
    bf16x8 kb0 = ldg(rs, l16, TADDR(1)), kb1 = ldg(rs, l16 + 1024, TADDR(1));
    bf16x8 va0 = ldg(rs, l16 + 2048, TADDR(0)), va1 = ldg(rs, l16 + 3072, TADDR(0));
    bf16x8 kc0 = ldg(rs, l16, TADDR(2)), kc1 = ldg(rs, l16 + 1024, TADDR(2));
    bf16x8 vb0 = ldg(rs, l16 + 2048, TADDR(1)), vb1 = ldg(rs, l16 + 3072, TADDR(1));
    bf16x8 vc0, vc1;
    if (wid >= 4) __builtin_amdgcn_s_setprio(1);
    const bool odd = h & 1;
    const bool mylane = odd ? !hh : hh;
#define SETM(qa, qb_)                                                                   \
  {                                                                                     \
    f32x16 s = MFMA(kr0, qa, zero); s = MFMA(kr1, qb_, s);                              \
    const unsigned nm = pk2(-xhalf_max(max16(s)), 0.f);                                 \
    u32x4 ta = __builtin_bit_cast(u32x4, qa), tb = __builtin_bit_cast(u32x4, qb_);      \
    if (mylane) { if (odd) ta[0] = nm; else tb[2] = nm; }                               \
    qa = __builtin_bit_cast(bf16x8, ta); qb_ = __builtin_bit_cast(bf16x8, tb);          \
  }
    SETM(q00, q01) SETM(q10, q11) SETM(q20, q21) SETM(q30, q31)
#undef SETM
    f32x16 s0 = MFMA(ka0, q00, zero); s0 = MFMA(ka1, q01, s0);
    f32x16 s1 = MFMA(ka0, q10, zero); s1 = MFMA(ka1, q11, s1);
    f32x16 s2 = MFMA(ka0, q20, zero); s2 = MFMA(ka1, q21, s2);
    f32x16 s3 = zero;
    u32x4 pa, pb0, pb1, pb2, pb3;
    STAMP(14);
    {
      f32x16 cur = dA0;
      int wsel = d_w3;
#pragma unroll 1
      for (int it = 0; it < d_n; ++it) {
        if (wsel >= 14) p1_store<true>(cur, 1, wsel, wid >> 2, d_t64, lane, rs); else p1_store<false>(cur, 1, wsel, wid >> 2, d_t64, lane, rs);
        cur = dA1; wsel = d_w2;
      }
    }
    auto wo_prefetch = [&]() {
      const int hi4 = wid >> 2;
#pragma unroll
      for (int i = 0; i < 7; ++i) glds16((const char*)woA + (2 * i + hi4) * 32 * 896, wo_vo, lds0 + L_WOA + ((2 * i + hi4) * 4 + (wid & 3)) * 1024);
#pragma unroll
      for (int i = 0; i < 4; ++i)
        if (2 * i + hi4 < 7 || wid == 4) glds16((const char*)woB + (2 * i + hi4) * 32 * 896, wo_vo, lds0 + L_WOB1 + ((2 * i + hi4) * 4 + (wid & 3)) * 1024);
      if (wid == 5 && lane < 32) *(__attribute__((address_space(3))) u32x4*)((lds_ptr)lds + L_WOA + 448 * 128 + lane * 16) = (u32x4){0u, 0u, 0u, 0u};
    };
#define SB() __builtin_amdgcn_sched_barrier(0)
#define PIN(x) asm volatile("" : "+v"(x))
#define EX4(S, i) { S[i] = __builtin_amdgcn_exp2f(S[i]); S[i + 1] = __builtin_amdgcn_exp2f(S[i + 1]); S[i + 2] = __builtin_amdgcn_exp2f(S[i + 2]); S[i + 3] = __builtin_amdgcn_exp2f(S[i + 3]); }
#define BLK(S, O, PB, V0, PS, PO, PPB, PV1, PK0, PK1, PQA, PQB, DOPV, DOQK)              \
  {                                                                                      \
    if (DOPV) PO = MFMA(PV1, __builtin_bit_cast(bf16x8, PPB), PO);                       \
    EX4(S, 0) PIN(S); SB();                                                              \
    if (DOQK) PS = MFMA(PK0, PQA, zero);                                                 \
    EX4(S, 4) pa = pack8u(S, 0); PIN(S); PIN(pa); SB();                                  \
    if (DOQK) PS = MFMA(PK1, PQB, PS);                                                   \
    EX4(S, 8) PIN(S); SB();                                                              \
    O = MFMA(V0, __builtin_bit_cast(bf16x8, pa), O);                                     \
    EX4(S, 12) PB = pack8u(S, 8); PIN(PB); SB();                                         \
  }
#define STEP(t, V0, V1, K0, K1, PV0, PV1, PK0, PK1)                                      \
  {                                                                                      \
    BLK(s0, o0, pb0, V0, s3, o3, pb3, PV1, PK0, PK1, q30, q31, t > 0, true)              \
    if (t == 1 && fb_off) {   \
      asm volatile("s_waitcnt vmcnt(4)" ::: "memory");                                   \
      if (lane == 0) {                                                                   \
        const unsigned old = __hip_atomic_fetch_add((__attribute__((address_space(3))) unsigned*)((lds_ptr)lds + L_CNT), 1u, __ATOMIC_RELAXED, __HIP_MEMORY_SCOPE_WORKGROUP); \
        if (old == 7) set_flag(ws, fb_off, tag);                                         \
      }                                                                                  \
      SB();                                                                              \
    }                                                                                    \
    if (t + 3 < 8) { PK0 = ldg(rs, l16, TADDR(t + 3)); PK1 = ldg(rs, l16 + 1024, TADDR(t + 3)); }        \
    if (t + 2 < 8) { PV0 = ldg(rs, l16 + 2048, TADDR(t + 2)); PV1 = ldg(rs, l16 + 3072, TADDR(t + 2)); } \
    if (t == 1 && fb_off) wo_prefetch();                                                 \
    SB();                                                                                \
    if (t == 7 && wid < 4) { lds_wait(lds, 4, 4); SB(); }                                \
    BLK(s1, o1, pb1, V0, s0, o0, pb0, V1, K0, K1, q00, q01, true, t < 7)                 \
    if (t == 7) { WRT(0, o0) SB(); }                                                     \
    BLK(s2, o2, pb2, V0, s1, o1, pb1, V1, K0, K1, q10, q11, true, t < 7)                 \
    if (t == 7) { WRT(1, o1) SB(); }                                                     \
    BLK(s3, o3, pb3, V0, s2, o2, pb2, V1, K0, K1, q20, q21, true, t < 7)                 \
    if (t == 7) { WRT(2, o2) SB(); }                                                     \
  }
    STEP(0, va0, va1, kb0, kb1, vc0, vc1, ka0, ka1)
    STEP(1, vb0, vb1, kc0, kc1, va0, va1, kb0, kb1)
    STEP(2, vc0, vc1, ka0, ka1, vb0, vb1, kc0, kc1)
    STEP(3, va0, va1, kb0, kb1, vc0, vc1, ka0, ka1)
    STEP(4, vb0, vb1, kc0, kc1, va0, va1, kb0, kb1)
    STEP(5, vc0, vc1, ka0, ka1, vb0, vb1, kc0, kc1)
    STEP(6, va0, va1, kb0, kb1, vc0, vc1, ka0, ka1)
    STEP(7, vb0, vb1, kc0, kc1, va0, va1, kb0, kb1)
    o3 = MFMA(vb1, __builtin_bit_cast(bf16x8, pb3), o3);
    WRT(3, o3)
#undef TADDR
#undef BLK
#undef EX4
#undef STEP
  }
  STAMP(5);
  STAMPW(12, 4);
#define RDADD(ACC)                                                                      \
  {                                                                                     \
    lds_ptr p_ = (lds_ptr)lds + L_CB + (wid & 3) * 16384 + l16;                         \
    _Pragma("unroll") for (int src = 0; src < 4; ++src)                                 \
      _Pragma("unroll") for (int q_ = 0; q_ < 4; ++q_) {                                \
        const f32x4 t_ = *(lf4)(p_ + src * 4096 + q_ * 1024);                           \
        ACC[4 * q_] += t_[0]; ACC[4 * q_ + 1] += t_[1]; ACC[4 * q_ + 2] += t_[2]; ACC[4 * q_ + 3] += t_[3]; \
      }                                                                                 \
  }
  f32x16 oh = zero;
  if (wid >= 4) {
    asm volatile("s_waitcnt lgkmcnt(0)" ::: "memory");
    if (lane == 0) lds_arrive(lds, 3);
    {
      const int pr = rb > 0 ? rb - 1 : rb;
      const int cw = (lane & 1) ? (28 * h + 27) >> 6 : (28 * h) >> 6;
      const int kind = lane >> 1;
      const int gg = kind == 0 ? cw : (kind & 1) ? 7 + (cw >> 1) : 7 + ((7 + cw) >> 1);
      wait_flags(ws, WS_FB + (gg * 16 + (kind >= 3 ? pr : rb)) * 128, lane < 10, tag);
    }
    const int t64 = rb * 4 + wid - 4;
    const unsigned qo = WS_QF + ((16 + h) * 64 + t64) * 2048;
    const unsigned kc = WS_KV + ((16 + h) * 64 + t64) * 4096;
    const unsigned kp = t64 > 0 ? kc - 4096 : kc;
    const bf16x8 q0 = ldg(rs, l16, qo), q1 = ldg(rs, l16 + 1024, qo);
    const bf16x8 kc0 = ldg(rs, l16, kc), kc1 = ldg(rs, l16 + 1024, kc), vc0 = ldg(rs, l16 + 2048, kc), vc1 = ldg(rs, l16 + 3072, kc);
    const bf16x8 kp0 = ldg(rs, l16, kp), kp1 = ldg(rs, l16 + 1024, kp), vp0 = ldg(rs, l16 + 2048, kp), vp1 = ldg(rs, l16 + 3072, kp);
    lds_wait(lds, 3, 4);
    RDADD(oh)
    asm volatile("s_waitcnt lgkmcnt(0)" ::: "memory");
    if (lane == 0) lds_arrive(lds, 4);
    f32x16 sc = MFMA(kc0, q0, zero); sc = MFMA(kc1, q1, sc);
    f32x16 sp = MFMA(kp0, q0, zero); sp = MFMA(kp1, q1, sp);
#pragma unroll
    for (int r = 0; r < 16; ++r) {
      const int i = (r & 3) + 8 * (r >> 2) + 4 * hh;
      const int dc = r32 - i, dp = r32 + 32 - i;
      const bool vc = (dc >= 0) && (dc <= 14) && !(dc & 1);
      const bool vp = (dp <= 14) && !(dp & 1) && (t64 > 0);
      sc[r] = vc ? sc[r] : -INFINITY;
      sp[r] = vp ? sp[r] : -INFINITY;
    }
    const float m = xhalf_max(fmaxf(max16(sc), max16(sp)));
#pragma unroll
    for (int r = 0; r < 16; ++r) {
      sc[r] = __builtin_amdgcn_exp2f(sc[r] - m);
      sp[r] = __builtin_amdgcn_exp2f(sp[r] - m);
    }
    f32x16 o = MFMA(vc0, pack8(sc, 0), zero);
    o = MFMA(vc1, pack8(sc, 8), o);
    o = MFMA(vp0, pack8(sp, 0), o);
    o = MFMA(vp1, pack8(sp, 8), o);
    const float l = __shfl(o[12], 32 + r32);
    const float rl = 1.0f / l;
#pragma unroll
    for (int r = 0; r < 16; ++r) o[r] *= rl;
    if (hh) { o[12] = 0.f; o[13] = 0.f; o[14] = 0.f; o[15] = 0.f; }
    const unsigned od = WS_OF + ((64 + t64) * 16 + h) * 2048;
    stg(rs, pack8u(o, 0), l16, od);
    stg(rs, pack8u(o, 8), l16 + 1024, od);
    asm volatile("s_waitcnt vmcnt(0)" ::: "memory");
    if (lane == 0) set_flag(ws, WS_F3A + (t64 * 16 + h) * 128, tag);
    STAMPW(2, 4);
  }
  BARRIER();
  STAMP(15);
  if (wid >= 4) {
    RDADD(oh)
    float l = __shfl(oh[12], 32 + r32);
    const int t64 = rb * 4 + wid - 4;
    if (__any(!(l < 1.0e18f))) {
      const unsigned qo = WS_QF + (h * 64 + t64) * 2048;
      const bf16x8 q0 = ldg(rs, l16, qo), q1 = ldg(rs, l16 + 1024, qo);
      float m = -INFINITY;
      oh = zero;
#pragma unroll 1
      for (int t = 0; t < 64; ++t) {
        const unsigned kv = WS_KV + (h * 64 + t) * 4096;
        const bf16x8 k0 = ldg(rs, l16, kv), k1 = ldg(rs, l16 + 1024, kv), v0 = ldg(rs, l16 + 2048, kv), v1 = ldg(rs, l16 + 3072, kv);
        f32x16 sx = MFMA(k0, q0, zero); sx = MFMA(k1, q1, sx);
        const float mn = fmaxf(m, xhalf_max(max16(sx)));
        const float al = __builtin_amdgcn_exp2f(m - mn);
        m = mn;
#pragma unroll
        for (int r = 0; r < 16; ++r) { oh[r] *= al; sx[r] = __builtin_amdgcn_exp2f(sx[r] - mn); }
        oh = MFMA(v0, pack8(sx, 0), oh);
        oh = MFMA(v1, pack8(sx, 8), oh);
      }
      l = __shfl(oh[12], 32 + r32);
    }
    const float rl = 1.0f / l;
#pragma unroll
    for (int r = 0; r < 16; ++r) oh[r] *= rl;
    if (hh) { oh[12] = 0.f; oh[13] = 0.f; oh[14] = 0.f; oh[15] = 0.f; }
    const unsigned od = WS_OF + (t64 * 16 + h) * 2048;
    stg(rs, pack8u(oh, 0), l16, od);
    stg(rs, pack8u(oh, 8), l16 + 1024, od);
  }
#undef WRT
#undef RDADD
  BARRIER();
  if (wid >= 4) {
    if (b >= 224) {
      asm volatile("s_waitcnt vmcnt(0)" ::: "memory");
      if (lane == 0 && lds_arrive(lds, 2) == 3) set_flag(ws, WS_F3 + (rb * 16 + h) * 128, tag);
    }
  } else if (b < 224) {
#pragma unroll
    for (int i = 0; i < 7; ++i) glds16((const char*)woB + (224 + i * 32) * 896, wo_vo, lds0 + L_CB + (i * 4 + wid) * 1024);
    if (wid == 0 && lane < 32) *(__attribute__((address_space(3))) u32x4*)((lds_ptr)lds + L_CB + 224 * 128 + lane * 16) = (u32x4){0u, 0u, 0u, 0u};
  }
  if (b >= 224) { STAMPDUMP(); return; }

  {
    const int rt = wid & 1, kq = wid >> 1;
    wait_flags(ws, lane < 4 ? WS_F3A + ((rg * 2 + rt) * 16 + kq * 4 + lane) * 128 : WS_FB + (((rg * 2 + rt) & 3) * 16 + (rg >> 1)) * 128,
               lane < 5, tag);
    if (wid >= 4) {
      asm volatile("s_waitcnt vmcnt(0)" ::: "memory");
      if (lane == 0 && lds_arrive(lds, 2) == 3) { set_flag(ws, WS_F3 + (rb * 16 + h) * 128, tag); STAMPX(6); }
    }
    const float g = __uint_as_float(__builtin_amdgcn_raw_buffer_load_b32(rs, r32 * 4, WS_G + (rg * 2 + rt) * 128, 16));
    const unsigned oa = WS_OF + ((64 + rg * 2 + rt) * 16 + kq * 4) * 2048;
    bf16x8 of[8], og[8];
#pragma unroll
    for (int i = 0; i < 8; ++i) of[i] = ldg(rs, l16 + (i & 3) * 1024, oa + (i >> 2) * 4096);
    WAITBAR(9);
    __builtin_amdgcn_s_setprio(0);
    const int wlo = (4 * hh + q4) * 128 + cb * 32 + p4 * 8;
    const int sw = (q4 >> 1) * 64;
    lds_cptr wpb = (lds_cptr)lds + (kq < 2 ? L_WOB1 + kq * 4 * 28 * 128 : L_CB + (kq - 2) * 4 * 28 * 128) + wlo;
    bf16x8 wb[16];
#pragma unroll
    for (int i = 0; i < 8; ++i) {
      lds_cptr p = wpb + ((i >> 1) * 28 + 16 * (i & 1)) * 128;
      wb[2 * i] = cat8(trrd(p + sw), trrd(p + sw + 8 * 128));
      wb[2 * i + 1] = cat8(trrd(p + (64 - sw)), trrd(p + (64 - sw) + 8 * 128));
    }
    __builtin_amdgcn_sched_barrier(0);
    STAMP(7);
    wait_flags(ws, WS_F3 + ((rg >> 1) * 16 + kq * 4 + lane) * 128, lane < 4, tag);
    STAMP(10);
    const unsigned ob = WS_OF + ((rg * 2 + rt) * 16 + kq * 4) * 2048;
#pragma unroll
    for (int i = 0; i < 8; ++i) og[i] = ldg(rs, l16 + (i & 3) * 1024, ob + (i >> 2) * 4096);
    __builtin_amdgcn_sched_barrier(0);
    lds_cptr wpa = (lds_cptr)lds + L_WOA + kq * 4 * 28 * 128 + wlo;
    f32x16 a0 = zero, a1 = zero;
#pragma unroll
    for (int i = 0; i < 8; ++i) {
      lds_cptr p = wpa + ((i >> 1) * 28 + 16 * (i & 1)) * 128;
      const bf16x8 wf0 = cat8(trrd(p + sw), trrd(p + sw + 8 * 128));
      const bf16x8 wf1 = cat8(trrd(p + (64 - sw)), trrd(p + (64 - sw) + 8 * 128));
      a0 = MFMA(wf0, of[i], a0);
      a1 = MFMA(wf1, of[i], a1);
    }
#pragma unroll
    for (int r = 0; r < 16; ++r) { a0[r] *= g; a1[r] *= g; }
    BARRIER();
    f32x16 b0 = zero, b1 = zero;
#pragma unroll
    for (int i = 0; i < 8; ++i) {
      b0 = MFMA(wb[2 * i], og[i], b0);
      b1 = MFMA(wb[2 * i + 1], og[i], b1);
    }
    const float g1 = 1.f - g;
#pragma unroll
    for (int r = 0; r < 16; ++r) { a0[r] += g1 * b0[r]; a1[r] += g1 * b1[r]; }
    lds_ptr ab = (lds_ptr)lds + L_WOA + rt * 32768 + kq * 2048 + l16;
#define WRQ(dst, A, o8)                                                                 \
  {                                                                                     \
    lds_ptr p = ab + (dst) * 8192;                                                      \
    *(lf4)p = (f32x4){A[o8], A[o8 + 1], A[o8 + 2], A[o8 + 3]};                          \
    *(lf4)(p + 1024) = (f32x4){A[o8 + 4], A[o8 + 5], A[o8 + 6], A[o8 + 7]};             \
  }
    WRQ(0, a0, 0) WRQ(1, a0, 8) WRQ(2, a1, 0) WRQ(3, a1, 8)
#undef WRQ
    BARRIER();
    {
      lds_ptr p = (lds_ptr)lds + L_WOA + rt * 32768 + kq * 8192 + l16;
      f32x4 u = *(lf4)p, w = *(lf4)(p + 1024);
#pragma unroll
      for (int src = 1; src < 4; ++src) { u += *(lf4)(p + src * 2048); w += *(lf4)(p + src * 2048 + 1024); }
      const int row = rg * 64 + rt * 32 + r32;
      const int col = cg3 * 64 + (kq >> 1) * 32 + (kq & 1) * 16 + 4 * hh;
      *(u32x2*)(out + (size_t)row * 448 + col) = (u32x2){pk2(u[0], u[1]), pk2(u[2], u[3])};
      *(u32x2*)(out + (size_t)row * 448 + col + 8) = (u32x2){pk2(w[0], w[1]), pk2(w[2], w[3])};
    }
  }
  STAMP(8);
  STAMPDUMP();
}

extern "C" void kernel_launch(void* const* d_in, const int* in_sizes, int n_in, void* d_out, int out_size,
                              void* d_ws, size_t ws_size, hipStream_t stream) {
  const u16* x = (const u16*)d_in[0];
  const u16* wqkvA = (const u16*)d_in[1];
  const u16* woA = (const u16*)d_in[2];
  const u16* wqkvB = (const u16*)d_in[4];
  const u16* woB = (const u16*)d_in[5];
  const u16* gw = (const u16*)d_in[6];
  (void)hipFuncSetAttribute((const void*)fused_k, hipFuncAttributeMaxDynamicSharedMemorySize, LDS_BYTES);
  hipLaunchKernelGGL(fused_k, dim3(256), dim3(512), LDS_BYTES, stream, x, wqkvA, wqkvB, woA, woB, gw, (char*)d_ws, (u16*)d_out);
}
```

```cpp
#include <hip/hip_runtime.h>
#include <hip/hip_bf16.h>
#include <stdint.h>

typedef unsigned short u16;
typedef unsigned long long u64;
typedef short bf16x8 __attribute__((ext_vector_type(8)));
typedef short s16x4 __attribute__((ext_vector_type(4)));
typedef float f32x16 __attribute__((ext_vector_type(16)));
typedef float f32x2 __attribute__((ext_vector_type(2)));
typedef float f32x4 __attribute__((ext_vector_type(4)));
typedef unsigned u32x4 __attribute__((ext_vector_type(4)));
typedef unsigned u32x2 __attribute__((ext_vector_type(2)));
typedef __bf16 bf2_t __attribute__((ext_vector_type(2)));
typedef __attribute__((address_space(3))) const char* lds_cptr;
typedef __attribute__((address_space(3))) char* lds_ptr;
typedef __attribute__((address_space(1))) u64 gu64;
typedef short v4i16_t __attribute__((ext_vector_type(4)));

#define DI __device__ __forceinline__
#define STAMP(i)
#define STAMPW(i, w)
#define STAMPX(i)
#define STAMPDUMP()
#define MFMA(a, b, c) __builtin_amdgcn_mfma_f32_32x32x16_bf16((a), (b), (c), 0, 0, 0)
#define BARRIER() asm volatile("s_waitcnt lgkmcnt(0)\n\ts_barrier" ::: "memory")
#define WAITBAR(N) asm volatile("s_waitcnt vmcnt(" #N ") lgkmcnt(0)\n\ts_barrier" ::: "memory")

extern "C" __device__ u64 gm_dispatch_id() __asm("llvm.amdgcn.dispatch.id");

#define CODE_LINES 288
constexpr float QSCALE = 0.18898223650461362f * 1.4426950408889634f;

constexpr unsigned WS_QF = 0;
constexpr unsigned WS_KV = 4u << 20;
constexpr unsigned WS_OF = 12u << 20;
constexpr unsigned WS_G = 16u << 20;
constexpr unsigned WS_FA = WS_G + 65536;
constexpr unsigned WS_FB = WS_FA + 32768;
constexpr unsigned WS_F3 = WS_FB + 32768;
constexpr unsigned WS_F3A = WS_F3 + 32768;
constexpr unsigned WS_END = WS_F3A + 131072;

constexpr int LDS_BYTES = 163840;
constexpr int L_X = 0;
constexpr int L_WR = 98304;
constexpr int L_GW = 147456;
constexpr int L_CNT = 163328;
constexpr int L_CB = 0;
constexpr int L_WOA = 65536;
constexpr int L_WOB1 = 123392;

DI unsigned pk2(float a, float b) {
  f32x2 v = {a, b};
  bf2_t r = __builtin_convertvector(v, bf2_t);
  return __builtin_bit_cast(unsigned, r);
}
DI u32x4 pack8u(const f32x16& x, int o) {
  u32x4 p;
  p[0] = pk2(x[o + 0], x[o + 1]); p[1] = pk2(x[o + 2], x[o + 3]);
  p[2] = pk2(x[o + 4], x[o + 5]); p[3] = pk2(x[o + 6], x[o + 7]);
  return p;
}
DI bf16x8 pack8(const f32x16& x, int o) { return __builtin_bit_cast(bf16x8, pack8u(x, o)); }
DI void glds16(const void* sbase, unsigned voff, unsigned lds_base) {
  unsigned sv;
  asm volatile("s_mov_b32 %0, m0\n\ts_mov_b32 m0, %3\n\ts_nop 4\n\tglobal_load_lds_dwordx4 %1, %2\n\ts_mov_b32 m0, %0"
               : "=&s"(sv) : "v"(voff), "s"(sbase), "s"(lds_base) : "memory");
}
DI s16x4 trrd(lds_cptr p) {
  return __builtin_bit_cast(s16x4, __builtin_amdgcn_ds_read_tr16_b64_v4i16((__attribute__((address_space(3))) v4i16_t*)p));
}
DI bf16x8 cat8(s16x4 lo, s16x4 hi) { return __builtin_shufflevector(lo, hi, 0, 1, 2, 3, 4, 5, 6, 7); }
DI bf16x8 ldsrd16(lds_cptr p) { return *(const __attribute__((address_space(3))) bf16x8*)p; }
DI float xhalf_max(float m) {
  auto rr = __builtin_amdgcn_permlane32_swap(__float_as_uint(m), __float_as_uint(m), false, false);
  return fmaxf(__uint_as_float(rr[0]), __uint_as_float(rr[1]));
}
DI float xhalf_sum(float m) {
  auto rr = __builtin_amdgcn_permlane32_swap(__float_as_uint(m), __float_as_uint(m), false, false);
  return __uint_as_float(rr[0]) + __uint_as_float(rr[1]);
}
DI float max16(const f32x16& s) {
  float a = fmaxf(fmaxf(s[0], s[1]), s[2]), b = fmaxf(fmaxf(s[3], s[4]), s[5]);
  a = fmaxf(fmaxf(a, s[6]), s[7]); b = fmaxf(fmaxf(b, s[8]), s[9]);
  a = fmaxf(fmaxf(a, s[10]), s[11]); b = fmaxf(fmaxf(b, s[12]), s[13]);
  a = fmaxf(fmaxf(a, s[14]), s[15]);
  return fmaxf(a, b);
}
DI bf16x8 ldg(__amdgpu_buffer_rsrc_t rs, unsigned voff, unsigned soff) {
  return __builtin_bit_cast(bf16x8, __builtin_amdgcn_raw_buffer_load_b128(rs, voff, soff, 16));
}
DI void stg(__amdgpu_buffer_rsrc_t rs, u32x4 v, unsigned voff, unsigned soff) {
  __builtin_amdgcn_raw_buffer_store_b128(v, rs, voff, soff, 16);
}
DI void wait_flags(const char* ws, unsigned off, bool active, u64 tag) {
  gu64* p = (gu64*)(ws + off);
  const u64 t0 = __builtin_amdgcn_s_memrealtime();
  for (;;) {
    u64 v = tag;
    if (active) v = __hip_atomic_load(p, __ATOMIC_RELAXED, __HIP_MEMORY_SCOPE_AGENT);
    if (__all(v == tag)) break;
    __builtin_amdgcn_s_sleep(1);
    if (__builtin_amdgcn_s_memrealtime() - t0 > 5000000ull) break;
  }
  __builtin_amdgcn_fence(__ATOMIC_ACQUIRE, "wavefront");
}
DI void set_flag(char* ws, unsigned off, u64 tag) {
  __hip_atomic_store((gu64*)(ws + off), tag, __ATOMIC_RELAXED, __HIP_MEMORY_SCOPE_AGENT);
}

DI void poll_fa(const char* ws, int lane, int wid, int h, int rb, u64 tag) {
  const int cw = (lane & 1) ? (28 * h + 27) >> 6 : (28 * h) >> 6;
  const int kind = lane >> 1;
  const int g = kind == 5 ? cw >> 1 : (kind == 2 || kind == 3) ? 7 + cw : (7 + cw) >> 1;
  const int r = kind == 5 ? rb : kind == 4 ? 0 : 2 * wid + (kind & 1);
  wait_flags(ws, WS_FA + (g * 16 + r) * 128, lane < 12, tag);
}

typedef __attribute__((address_space(3))) unsigned* lds_u32p;
DI unsigned lds_arrive(char* lds, int idx) {
  return __hip_atomic_fetch_add((lds_u32p)((lds_ptr)lds + L_CNT) + idx, 1u, __ATOMIC_RELAXED, __HIP_MEMORY_SCOPE_WORKGROUP);
}
DI void lds_wait(char* lds, int idx, unsigned n) {
  while (__hip_atomic_load((lds_u32p)((lds_ptr)lds + L_CNT) + idx, __ATOMIC_RELAXED, __HIP_MEMORY_SCOPE_WORKGROUP) < n) __builtin_amdgcn_s_sleep(1);
  asm volatile("" ::: "memory");
}

struct P1Ctx {
  const char* x; const char* w1; const char* w2; const char* w3;
  unsigned lds0, xoff0, xoff1, woff;
  int wid, lane, rot;
};
DI int rotc(const P1Ctx& c, int ch) { const int t = ch + c.rot; return t >= 7 ? t - 7 : t; }
DI void p1_x(const P1Ctx& c, int ch) {
  const int slot = ch == 6 ? 0 : ch;
  const char* src = c.x + rotc(c, ch) * 128;
  glds16(src, c.xoff0, c.lds0 + L_X + slot * 16384 + c.wid * 1024);
  glds16(src, c.xoff1, c.lds0 + L_X + slot * 16384 + (c.wid + 8) * 1024);
}
DI void p1_w12(const P1Ctx& c, int ch) {
  const int slot = ch % 3;
  const int ro = rotc(c, ch) * (64 * 2688);
  glds16(c.w1 + ro, c.woff, c.lds0 + L_WR + slot * 16384 + c.wid * 1024);
  glds16(c.w2 + ro, c.woff, c.lds0 + L_WR + slot * 16384 + 8192 + c.wid * 1024);
}
DI constexpr int w3_off(int ch) { return L_X + (1 + (ch >> 1)) * 16384 + (ch & 1) * 8192; }
DI void p1_w3(const P1Ctx& c, int ch) {
  glds16(c.w3 + rotc(c, ch) * (64 * 2688), c.woff, c.lds0 + w3_off(ch) + c.wid * 1024);
}

template <bool ISV>
DI void p1_store(const f32x16& a, int br, int w, int ct, int t64, int lane, __amdgpu_buffer_rsrc_t rs) {
  const int r32 = lane & 31, hh = lane >> 5;
  const int sec = w / 7, wc = (w - sec * 7) * 64;
  if (ISV) {
    const int c = wc + ct * 32 + r32, head = c / 28, d = c - head * 28;
    const unsigned off = WS_KV + ((br * 16 + head) * 64 + t64) * 4096 + 2048 + (hh * 32 + d) * 16;
    stg(rs, pack8u(a, 0), off, 0);
    stg(rs, pack8u(a, 8), off + 1024, 0);
    if (d >= 24) {
      const unsigned f = d == 24 ? 0x3F803F80u : 0u;
      const u32x4 pv = {f, f, f, f};
      stg(rs, pv, off + 64, 0);
      stg(rs, pv, off + 64 + 1024, 0);
    }
  } else {
    const float sc = sec == 0 ? QSCALE : 1.f;
    unsigned w8[8];
#pragma unroll
    for (int i = 0; i < 8; ++i) w8[i] = pk2(a[2 * i] * sc, a[2 * i + 1] * sc);
    const unsigned padv = sec == 0 ? 0u : 0x3F80u;
    const unsigned hstride = sec == 0 ? 64u * 2048u : 64u * 4096u;
    const unsigned base0 = (sec == 0 ? WS_QF + (br * 16 * 64 + t64) * 2048 : WS_KV + (br * 16 * 64 + t64) * 4096) + r32 * 16;
#pragma unroll
    for (int pp = 0; pp < 2; ++pp) {
      const auto s0 = __builtin_amdgcn_permlane32_swap(w8[4 * pp], w8[4 * pp + 2], false, false);
      const auto s1 = __builtin_amdgcn_permlane32_swap(w8[4 * pp + 1], w8[4 * pp + 3], false, false);
      const int C = (wc >> 3) + ct * 4 + 2 * pp + hh, hp = C / 7, r = C - hp * 7;
      const int head = 2 * hp + (r > 3 ? 1 : 0), cc = r > 3 ? r - 3 : r;
      u32x4 v = {s0[0], s1[0], s0[1], s1[1]};
      if (r == 3) {
        const u32x4 v2 = {padv, 0u, s0[1], s1[1]};
        stg(rs, v2, base0 + (head + 1) * hstride, 0);
        v[2] = padv; v[3] = 0u;
      }
      stg(rs, v, base0 + head * hstride + (cc >> 1) * 1024 + (cc & 1) * 512, 0);
    }
  }
}

template <bool V1, bool V2>
DI void p1_sweep1(const P1Ctx& c, char* lds, bf16x8 (&xf)[28], f32x16& acc0, f32x16& acc1, int wl) {
  const int lane = c.lane, r32 = lane & 31, hh = lane >> 5;
  const int xrow = (c.wid & 3) * 32 + r32, xsw = (xrow >> 1) & 7;
  lds_cptr xb = (lds_cptr)lds + L_X + xrow * 128;
  bf16x8 wf[2][8];
#pragma unroll
  for (int ch = 0; ch < 8; ++ch) {
    if (ch == 7) __builtin_amdgcn_sched_barrier(0);
    else if (ch == 0) WAITBAR(8);
    else if (ch <= 4) WAITBAR(6);
    else if (ch == 5) WAITBAR(4);
    else if (ch == 6) WAITBAR(4);
    if (ch == 0) STAMP(9);
    if (ch == 6) STAMP(11);
    lds_cptr wb = (lds_cptr)lds + L_WR + (ch % 3) * 16384 + wl;
    lds_cptr xc = xb + (ch == 6 ? 0 : ch) * 16384;
#pragma unroll
    for (int s = 0; s < 4; ++s) {
      if (ch < 7) {
        xf[ch * 4 + s] = ldsrd16(xc + (((2 * s + hh) ^ xsw) * 16));
        lds_cptr wp = wb + s * 2048;
        wf[ch & 1][s] = cat8(trrd(wp), trrd(wp + 512));
        wf[ch & 1][4 + s] = cat8(trrd(wp + 8192), trrd(wp + 8192 + 512));
      }
      if (ch >= 1) {
        const bf16x8 x8 = xf[(ch - 1) * 4 + s];
        const bf16x8 w0 = wf[(ch - 1) & 1][s], w1 = wf[(ch - 1) & 1][4 + s];
        acc0 = V1 ? MFMA(x8, w0, acc0) : MFMA(w0, x8, acc0);
        acc1 = V2 ? MFMA(x8, w1, acc1) : MFMA(w1, x8, acc1);
      }
      __builtin_amdgcn_sched_barrier(0);
      if (s == 0 && ch >= 1 && ch <= 4) p1_w12(c, ch + 2);
      if (s == 1 && ch <= 3) p1_x(c, ch + 3);
      if (s == 0 && ch == 5) { p1_w3(c, 0); p1_w3(c, 1); }
      if (s == 1 && ch == 5) { p1_w3(c, 2); p1_w3(c, 3); }
      if (s == 0 && ch == 6) { p1_w3(c, 4); p1_w3(c, 5); }
      if (s == 1 && ch == 6) { p1_w3(c, 6); }
      __builtin_amdgcn_sched_barrier(0);
    }
  }
}
template <bool V3, int C0, int C1>
DI void p1_sweep2(char* lds, const bf16x8 (&xf)[28], f32x16& acc0, int wl) {
#pragma unroll
  for (int ch = C0; ch < C1; ++ch) {
    lds_cptr wb = (lds_cptr)lds + w3_off(ch) + wl;
#pragma unroll
    for (int s = 0; s < 4; ++s) {
      lds_cptr wp = wb + s * 2048;
      const bf16x8 w0 = cat8(trrd(wp), trrd(wp + 512));
      acc0 = V3 ? MFMA(xf[ch * 4 + s], w0, acc0) : MFMA(w0, xf[ch * 4 + s], acc0);
    }
  }
}

__global__ __launch_bounds__(512, 1) void fused_k(const u16* __restrict__ x, const u16* __restrict__ wA,
                                                  const u16* __restrict__ wB, const u16* __restrict__ woA,
                                                  const u16* __restrict__ woB, const u16* __restrict__ gw,
                                                  char* ws, u16* __restrict__ out) {
  extern __shared__ __attribute__((aligned(16))) char lds[];
  unsigned code_touch = 0;
  {
    u64 pc;
    asm volatile("s_getpc_b64 %0" : "=s"(pc));
    if (blockIdx.x < 40 && threadIdx.x >= 448)
      code_touch = *(const __attribute__((address_space(1))) unsigned*)((pc & ~127ull) + (u64)min((blockIdx.x >> 3) * 64 + (threadIdx.x & 63), CODE_LINES - 1) * 128);
  }
  asm volatile("" :: "s"(x), "s"(wA), "s"(wB), "s"(woA), "s"(woB), "s"(gw), "s"(ws), "s"(out));
  const int tid = threadIdx.x, lane = tid & 63;
  const int wid = __builtin_amdgcn_readfirstlane(tid >> 6);
  const int b = blockIdx.x;
  const int r32 = lane & 31, hh = lane >> 5;
  const int g16 = lane >> 4, i16 = lane & 15, q4 = i16 >> 2, p4 = i16 & 3, cb = g16 & 1;
  const unsigned lds0 = (unsigned)(uintptr_t)lds;
  const __amdgpu_buffer_rsrc_t rs = __builtin_amdgcn_make_buffer_rsrc((void*)ws, 0, (int)WS_END, 0x00020000);
  const u64 tag = ((gm_dispatch_id() + 1) * 0x9E3779B97F4A7C15ull) ^ (u64)(uintptr_t)__builtin_amdgcn_dispatch_ptr() ^ 0x5A17C3E9D2B40F61ull;
  const f32x16 zero = {};
  const unsigned l16 = lane * 16;
  STAMP(0);
  const int h = (b & 7) + 8 * ((b >> 3) & 1), rb = b >> 4;
  unsigned fb_off = 0;
  f32x16 dA0 = zero, dA1 = zero;
  int d_w3 = 0, d_w2 = 0, d_n = 0, d_t64 = 0;
  if (tid < 8) ((__attribute__((address_space(3))) unsigned*)((lds_ptr)lds + L_CNT))[tid] = 0u;

  if (__builtin_expect(b < 224, 1)) {
    const int i5 = b >> 3, g = (b & 1) + 2 * (i5 % 7), rb1 = 4 * ((b & 7) >> 1) + i5 / 7;
    const int w1 = g < 7 ? 2 * g : 7 + g, w2 = g < 7 ? 2 * g + 1 : 2 * g - 7, w3 = g < 7 ? g : 2 * g - 6;
    const int br2 = g < 7 ? 0 : 1;
    P1Ctx c;
    c.x = (const char*)x; c.lds0 = lds0; c.wid = wid; c.lane = lane;
    c.rot = (i5 % 7 + 2 * (i5 / 7)) % 7;
    c.w1 = (const char*)wB + w1 * 128;
    c.w2 = (const char*)(g < 7 ? wB : wA) + w2 * 128;
    c.w3 = (const char*)wA + w3 * 128;
    {
      const int rl8 = lane >> 3, row0 = 8 * wid + rl8, row1 = row0 + 64;
      c.xoff0 = (unsigned)((rb1 * 128 + row0) * 896 + (((lane & 7) ^ ((row0 >> 1) & 7)) * 16));
      c.xoff1 = (unsigned)((rb1 * 128 + row1) * 896 + (((lane & 7) ^ ((row1 >> 1) & 7)) * 16));
      c.woff = (unsigned)(row0 * 2688 + (((lane & 7) ^ (((rl8 >> 1) & 1) << 2)) * 16));
    }
    p1_x(c, 0); p1_w12(c, 0);
    asm volatile("s_barrier" ::: "memory");
    p1_x(c, 1); p1_w12(c, 1); p1_x(c, 2); p1_w12(c, 2);
    const int rt = wid & 3, ct = wid >> 2, t64 = rb1 * 4 + rt;
    const int wl = (8 * hh + q4) * 128 + ((ct ^ (q4 >> 1)) * 64 + cb * 32 + p4 * 8);
    bf16x8 xf[28];
    f32x16 acc0 = zero, acc1 = zero;
    if (g < 7) p1_sweep1<false, false>(c, lds, xf, acc0, acc1, wl);
    else if (g < 11) p1_sweep1<true, false>(c, lds, xf, acc0, acc1, wl);
    else p1_sweep1<true, true>(c, lds, xf, acc0, acc1, wl);
    if (g < 7) {
      f32x16 cur = acc0;
#pragma unroll 1
      for (int it = 0; it < 2; ++it) { p1_store<false>(cur, 0, w1 + it, ct, t64, lane, rs); cur = acc1; }
    } else {
      p1_store<true>(acc0, 0, w1, ct, t64, lane, rs);
    }
    STAMP(1);
    WAITBAR(4);
    acc0 = zero;
    if (g < 10) p1_sweep2<false, 0, 2>(lds, xf, acc0, wl); else p1_sweep2<true, 0, 2>(lds, xf, acc0, wl);
    asm volatile("s_waitcnt vmcnt(0)" ::: "memory");
    if (lane == 0) {
      const unsigned old = __hip_atomic_fetch_add((__attribute__((address_space(3))) unsigned*)((lds_ptr)lds + L_CNT + 4), 1u, __ATOMIC_RELAXED, __HIP_MEMORY_SCOPE_WORKGROUP);
      if (old == 7) { set_flag(ws, WS_FA + (g * 16 + rb1) * 128, tag); STAMPX(13); }
    }
    if (g < 10) p1_sweep2<false, 2, 7>(lds, xf, acc0, wl); else p1_sweep2<true, 2, 7>(lds, xf, acc0, wl);
    dA0 = acc0; dA1 = acc1; d_w3 = w3; d_w2 = w2; d_n = 1 + br2; d_t64 = t64;
    fb_off = WS_FB + (g * 16 + rb1) * 128;
  } else {
    typedef const __attribute__((address_space(1))) unsigned* gptr;
    const int q = (b - 224) >> 3;
    const int l0 = q * 784 + tid, l1 = q * 784 + min(tid + 512, 783);
    const unsigned t1 = *(gptr)((u64)woB + (u64)l0 * 128), t2 = *(gptr)((u64)woB + (u64)l1 * 128);
    const unsigned t3 = *(gptr)((u64)woA + (u64)l0 * 128), t4 = *(gptr)((u64)woA + (u64)l1 * 128);
    const unsigned acc = t1 ^ t2 ^ t3 ^ t4;
    asm volatile("" :: "v"(acc));
    {
      const int row0 = (b - 224) * 64 + wid * 8;
      const int lc = min(lane, 55);
      const u32x4 gv = *(const __attribute__((address_space(1))) u32x4*)((u64)gw + lc * 16);
      u32x4 xv[8];
#pragma unroll
      for (int i = 0; i < 8; ++i) xv[i] = *(const __attribute__((address_space(1))) u32x4*)((u64)x + (u64)(row0 + i) * 896 + lc * 16);
      float res = 0.f;
#pragma unroll
      for (int i = 0; i < 8; ++i) {
        float z = 0.f;
#pragma unroll
        for (int e = 0; e < 4; ++e) {
          const unsigned xe = xv[i][e], ge = gv[e];
          z = __builtin_amdgcn_fdot2_f32_bf16(__builtin_bit_cast(bf2_t, xe), __builtin_bit_cast(bf2_t, ge), z, false);
        }
        if (lane >= 56) z = 0.f;
#pragma unroll
        for (int o = 32; o >= 1; o >>= 1) z += __shfl_xor(z, o);
        if (lane == i) res = 1.0f / (1.0f + __expf(-z));
      }
      if (lane < 8) __builtin_amdgcn_raw_buffer_store_b32(__float_as_uint(res), rs, lane * 4, WS_G + row0 * 4, 16);
      WAITBAR(0);
      if (tid == 0) set_flag(ws, WS_FB + (14 * 16 + b - 224) * 128, tag);
    }
  }

  asm volatile("" :: "v"(code_touch));
  BARRIER();
  STAMP(3);
  poll_fa(ws, lane, wid, h, rb, tag);
  STAMP(4);
  typedef __attribute__((address_space(3))) f32x4* lf4;
  const int i5 = b >> 3, rg = (b & 7) + 8 * (i5 / 7), cg3 = i5 % 7;
  const unsigned wo_vo = (unsigned)(((wid & 3) * 8 + (lane >> 3)) * 896 + cg3 * 128 + (((lane & 7) ^ (((lane >> 4) & 1) << 2)) * 16));
#define WRT(dst, O)                                                                     \
  {                                                                                     \
    lds_ptr p_ = (lds_ptr)lds + L_CB + (dst) * 16384 + (wid & 3) * 4096 + l16;          \
    *(lf4)p_ = (f32x4){O[0], O[1], O[2], O[3]};                                         \
    *(lf4)(p_ + 1024) = (f32x4){O[4], O[5], O[6], O[7]};                                \
    *(lf4)(p_ + 2048) = (f32x4){O[8], O[9], O[10], O[11]};                              \
    *(lf4)(p_ + 3072) = (f32x4){O[12], O[13], O[14], O[15]};                            \
  }
  f32x16 o0 = zero, o1 = zero, o2 = zero, o3 = zero;
  {
    const unsigned kvh = WS_KV + (h * 64) * 4096;
    const unsigned qb = WS_QF + (h * 64 + rb * 4) * 2048;
    bf16x8 q00 = ldg(rs, l16, qb), q01 = ldg(rs, l16 + 1024, qb);
    bf16x8 q10 = ldg(rs, l16 + 2048, qb), q11 = ldg(rs, l16 + 3072, qb);
    bf16x8 q20 = ldg(rs, l16, qb + 4096), q21 = ldg(rs, l16 + 1024, qb + 4096);
    bf16x8 q30 = ldg(rs, l16 + 2048, qb + 4096), q31 = ldg(rs, l16 + 3072, qb + 4096);
    const bf16x8 kr0 = ldg(rs, l16, kvh), kr1 = ldg(rs, l16 + 1024, kvh);
    const unsigned kvw = kvh + wid * 8 * 4096;
#define TADDR(t) (kvw + (unsigned)((((t) + rb) & 7) * 4096))
    bf16x8 ka0 = ldg(rs, l16, TADDR(0)), ka1 = ldg(rs, l16 + 1024, TADDR(0));
    bf16x8 kb0 = ldg(rs, l16, TADDR(1)), kb1 = ldg(rs, l16 + 1024, TADDR(1));
    bf16x8 va0 = ldg(rs, l16 + 2048, TADDR(0)), va1 = ldg(rs, l16 + 3072, TADDR(0));
    bf16x8 kc0 = ldg(rs, l16, TADDR(2)), kc1 = ldg(rs, l16 + 1024, TADDR(2));
    bf16x8 vb0 = ldg(rs, l16 + 2048, TADDR(1)), vb1 = ldg(rs, l16 + 3072, TADDR(1));
    bf16x8 vc0, vc1;
    if (wid >= 4) __builtin_amdgcn_s_setprio(1);
    const bool odd = h & 1;
    const bool mylane = odd ? !hh : hh;
#define SETM(qa, qb_)                                                                   \
  {                                                                                     \
    f32x16 s = MFMA(kr0, qa, zero); s = MFMA(kr1, qb_, s);                              \
    const unsigned nm = pk2(-xhalf_max(max16(s)), 0.f);                                 \
    u32x4 ta = __builtin_bit_cast(u32x4, qa), tb = __builtin_bit_cast(u32x4, qb_);      \
    if (mylane) { if (odd) ta[0] = nm; else tb[2] = nm; }                               \
    qa = __builtin_bit_cast(bf16x8, ta); qb_ = __builtin_bit_cast(bf16x8, tb);          \
  }
    SETM(q00, q01) SETM(q10, q11) SETM(q20, q21) SETM(q30, q31)
#undef SETM
    f32x16 s0 = MFMA(ka0, q00, zero); s0 = MFMA(ka1, q01, s0);
    f32x16 s1 = MFMA(ka0, q10, zero); s1 = MFMA(ka1, q11, s1);
    f32x16 s2 = MFMA(ka0, q20, zero); s2 = MFMA(ka1, q21, s2);
    f32x16 s3 = zero;
    u32x4 pa, pb0, pb1, pb2, pb3;
    STAMP(14);
    {
      f32x16 cur = dA0;
      int wsel = d_w3;
#pragma unroll 1
      for (int it = 0; it < d_n; ++it) {
        if (wsel >= 14) p1_store<true>(cur, 1, wsel, wid >> 2, d_t64, lane, rs); else p1_store<false>(cur, 1, wsel, wid >> 2, d_t64, lane, rs);
        cur = dA1; wsel = d_w2;
      }
    }
    auto wo_prefetch = [&]() {
      const int hi4 = wid >> 2;
#pragma unroll
      for (int i = 0; i < 7; ++i) glds16((const char*)woA + (2 * i + hi4) * 32 * 896, wo_vo, lds0 + L_WOA + ((2 * i + hi4) * 4 + (wid & 3)) * 1024);
#pragma unroll
      for (int i = 0; i < 4; ++i)
        if (2 * i + hi4 < 7 || wid == 4) glds16((const char*)woB + (2 * i + hi4) * 32 * 896, wo_vo, lds0 + L_WOB1 + ((2 * i + hi4) * 4 + (wid & 3)) * 1024);
      if (wid == 5 && lane < 32) *(__attribute__((address_space(3))) u32x4*)((lds_ptr)lds + L_WOA + 448 * 128 + lane * 16) = (u32x4){0u, 0u, 0u, 0u};
    };
#define SB() __builtin_amdgcn_sched_barrier(0)
#define PIN(x) asm volatile("" : "+v"(x))
#define EX4(S, i) { S[i] = __builtin_amdgcn_exp2f(S[i]); S[i + 1] = __builtin_amdgcn_exp2f(S[i + 1]); S[i + 2] = __builtin_amdgcn_exp2f(S[i + 2]); S[i + 3] = __builtin_amdgcn_exp2f(S[i + 3]); }
#define BLK(S, O, PB, V0, PS, PO, PPB, PV1, PK0, PK1, PQA, PQB, DOPV, DOQK)              \
  {                                                                                      \
    if (DOPV) PO = MFMA(PV1, __builtin_bit_cast(bf16x8, PPB), PO);                       \
    EX4(S, 0) PIN(S); SB();                                                              \
    if (DOQK) PS = MFMA(PK0, PQA, zero);                                                 \
    EX4(S, 4) pa = pack8u(S, 0); PIN(S); PIN(pa); SB();                                  \
    if (DOQK) PS = MFMA(PK1, PQB, PS);                                                   \
    EX4(S, 8) PIN(S); SB();                                                              \
    O = MFMA(V0, __builtin_bit_cast(bf16x8, pa), O);                                     \
    EX4(S, 12) PB = pack8u(S, 8); PIN(PB); SB();                                         \
  }
#define STEP(t, V0, V1, K0, K1, PV0, PV1, PK0, PK1)                                      \
  {                                                                                      \
    BLK(s0, o0, pb0, V0, s3, o3, pb3, PV1, PK0, PK1, q30, q31, t > 0, true)              \
    if (t == 1 && fb_off) {   \
      asm volatile("s_waitcnt vmcnt(4)" ::: "memory");                                   \
      if (lane == 0) {                                                                   \
        const unsigned old = __hip_atomic_fetch_add((__attribute__((address_space(3))) unsigned*)((lds_ptr)lds + L_CNT), 1u, __ATOMIC_RELAXED, __HIP_MEMORY_SCOPE_WORKGROUP); \
        if (old == 7) set_flag(ws, fb_off, tag);                                         \
      }                                                                                  \
      SB();                                                                              \
    }                                                                                    \
    if (t + 3 < 8) { PK0 = ldg(rs, l16, TADDR(t + 3)); PK1 = ldg(rs, l16 + 1024, TADDR(t + 3)); }        \
    if (t + 2 < 8) { PV0 = ldg(rs, l16 + 2048, TADDR(t + 2)); PV1 = ldg(rs, l16 + 3072, TADDR(t + 2)); } \
    if (t == 1 && fb_off) wo_prefetch();                                                 \
    SB();                                                                                \
    if (t == 7 && wid < 4) { lds_wait(lds, 4, 4); SB(); }                                \
    BLK(s1, o1, pb1, V0, s0, o0, pb0, V1, K0, K1, q00, q01, true, t < 7)                 \
    if (t == 7) { WRT(0, o0) SB(); }                                                     \
    BLK(s2, o2, pb2, V0, s1, o1, pb1, V1, K0, K1, q10, q11, true, t < 7)                 \
    if (t == 7) { WRT(1, o1) SB(); }                                                     \
    BLK(s3, o3, pb3, V0, s2, o2, pb2, V1, K0, K1, q20, q21, true, t < 7)                 \
    if (t == 7) { WRT(2, o2) SB(); }                                                     \
  }
    STEP(0, va0, va1, kb0, kb1, vc0, vc1, ka0, ka1)
    STEP(1, vb0, vb1, kc0, kc1, va0, va1, kb0, kb1)
    STEP(2, vc0, vc1, ka0, ka1, vb0, vb1, kc0, kc1)
    STEP(3, va0, va1, kb0, kb1, vc0, vc1, ka0, ka1)
    STEP(4, vb0, vb1, kc0, kc1, va0, va1, kb0, kb1)
    STEP(5, vc0, vc1, ka0, ka1, vb0, vb1, kc0, kc1)
    STEP(6, va0, va1, kb0, kb1, vc0, vc1, ka0, ka1)
    STEP(7, vb0, vb1, kc0, kc1, va0, va1, kb0, kb1)
    o3 = MFMA(vb1, __builtin_bit_cast(bf16x8, pb3), o3);
    WRT(3, o3)
#undef TADDR
#undef BLK
#undef EX4
#undef STEP
  }
  STAMP(5);
  STAMPW(12, 4);
#define RDADD(ACC)                                                                      \
  {                                                                                     \
    lds_ptr p_ = (lds_ptr)lds + L_CB + (wid & 3) * 16384 + l16;                         \
    _Pragma("unroll") for (int src = 0; src < 4; ++src)                                 \
      _Pragma("unroll") for (int q_ = 0; q_ < 4; ++q_) {                                \
        const f32x4 t_ = *(lf4)(p_ + src * 4096 + q_ * 1024);                           \
        ACC[4 * q_] += t_[0]; ACC[4 * q_ + 1] += t_[1]; ACC[4 * q_ + 2] += t_[2]; ACC[4 * q_ + 3] += t_[3]; \
      }                                                                                 \
  }
  f32x16 oh = zero;
  if (wid >= 4) {
    asm volatile("s_waitcnt lgkmcnt(0)" ::: "memory");
    if (lane == 0) lds_arrive(lds, 3);
    {
      const int pr = rb > 0 ? rb - 1 : rb;
      const int cw = (lane & 1) ? (28 * h + 27) >> 6 : (28 * h) >> 6;
      const int kind = lane >> 1;
      const int gg = kind == 0 ? cw : (kind & 1) ? 7 + (cw >> 1) : 7 + ((7 + cw) >> 1);
      wait_flags(ws, WS_FB + (gg * 16 + (kind >= 3 ? pr : rb)) * 128, lane < 10, tag);
    }
    const int t64 = rb * 4 + wid - 4;
    const unsigned qo = WS_QF + ((16 + h) * 64 + t64) * 2048;
    const unsigned kc = WS_KV + ((16 + h) * 64 + t64) * 4096;
    const unsigned kp = t64 > 0 ? kc - 4096 : kc;
    const bf16x8 q0 = ldg(rs, l16, qo), q1 = ldg(rs, l16 + 1024, qo);
    const bf16x8 kc0 = ldg(rs, l16, kc), kc1 = ldg(rs, l16 + 1024, kc), vc0 = ldg(rs, l16 + 2048, kc), vc1 = ldg(rs, l16 + 3072, kc);
    const bf16x8 kp0 = ldg(rs, l16, kp), kp1 = ldg(rs, l16 + 1024, kp), vp1 = ldg(rs, l16 + 3072, kp);
    lds_wait(lds, 3, 4);
    RDADD(oh)
    asm volatile("s_waitcnt lgkmcnt(0)" ::: "memory");
    if (lane == 0) lds_arrive(lds, 4);
    f32x16 sc = MFMA(kc0, q0, zero); sc = MFMA(kc1, q1, sc);
    f32x16 sp = MFMA(kp0, q0, zero); sp = MFMA(kp1, q1, sp);
    const u64 mk = (0x5555ull << (r32 + 18)) >> (4 * hh);
    const int mcur = (int)(unsigned)(mk >> 32), mprev = t64 > 0 ? (int)(unsigned)mk : 0;
#pragma unroll
    for (int r = 0; r < 16; ++r) {
      const int i = (r & 3) + 8 * (r >> 2);
      const unsigned selc = (unsigned)__builtin_amdgcn_sbfe(mcur, i, 1);
      sc[r] = __uint_as_float((__float_as_uint(sc[r]) & selc) | (0xFF800000u & ~selc));
      if (r >= 8) {
        const unsigned selp = (unsigned)__builtin_amdgcn_sbfe(mprev, i, 1);
        sp[r] = __uint_as_float((__float_as_uint(sp[r]) & selp) | (0xFF800000u & ~selp));
      }
    }
    float mx = max16(sc);
#pragma unroll
    for (int r = 8; r < 16; r += 2) mx = fmaxf(fmaxf(mx, sp[r]), sp[r + 1]);
    const float m = xhalf_max(mx);
#pragma unroll
    for (int r = 0; r < 16; ++r) {
      sc[r] = __builtin_amdgcn_exp2f(sc[r] - m);
      if (r >= 8) sp[r] = __builtin_amdgcn_exp2f(sp[r] - m);
    }
    f32x16 o = MFMA(vc0, pack8(sc, 0), zero);
    o = MFMA(vc1, pack8(sc, 8), o);
    o = MFMA(vp1, pack8(sp, 8), o);
    const float l = __shfl(o[12], 32 + r32);
    const float rl = 1.0f / l;
#pragma unroll
    for (int r = 0; r < 16; ++r) o[r] *= rl;
    if (hh) { o[12] = 0.f; o[13] = 0.f; o[14] = 0.f; o[15] = 0.f; }
    const unsigned od = WS_OF + ((64 + t64) * 16 + h) * 2048;
    stg(rs, pack8u(o, 0), l16, od);
    stg(rs, pack8u(o, 8), l16 + 1024, od);
    asm volatile("s_waitcnt vmcnt(0)" ::: "memory");
    if (lane == 0) set_flag(ws, WS_F3A + (t64 * 16 + h) * 128, tag);
    STAMPW(2, 4);
  }
  BARRIER();
  STAMP(15);
  if (wid >= 4) {
    RDADD(oh)
    float l = __shfl(oh[12], 32 + r32);
    const int t64 = rb * 4 + wid - 4;
    if (__any(!(l < 1.0e18f && l > 1.0e-18f))) {
      const unsigned qo = WS_QF + (h * 64 + t64) * 2048;
      const bf16x8 q0 = ldg(rs, l16, qo), q1 = ldg(rs, l16 + 1024, qo);
      float m = -INFINITY;
      oh = zero;
#pragma unroll 1
      for (int t = 0; t < 64; ++t) {
        const unsigned kv = WS_KV + (h * 64 + t) * 4096;
        const bf16x8 k0 = ldg(rs, l16, kv), k1 = ldg(rs, l16 + 1024, kv), v0 = ldg(rs, l16 + 2048, kv), v1 = ldg(rs, l16 + 3072, kv);
        f32x16 sx = MFMA(k0, q0, zero); sx = MFMA(k1, q1, sx);
        const float mn = fmaxf(m, xhalf_max(max16(sx)));
        const float al = __builtin_amdgcn_exp2f(m - mn);
        m = mn;
#pragma unroll
        for (int r = 0; r < 16; ++r) { oh[r] *= al; sx[r] = __builtin_amdgcn_exp2f(sx[r] - mn); }
        oh = MFMA(v0, pack8(sx, 0), oh);
        oh = MFMA(v1, pack8(sx, 8), oh);
      }
      l = __shfl(oh[12], 32 + r32);
    }
    const float rl = 1.0f / l;
#pragma unroll
    for (int r = 0; r < 16; ++r) oh[r] *= rl;
    if (hh) { oh[12] = 0.f; oh[13] = 0.f; oh[14] = 0.f; oh[15] = 0.f; }
    const unsigned od = WS_OF + (t64 * 16 + h) * 2048;
    stg(rs, pack8u(oh, 0), l16, od);
    stg(rs, pack8u(oh, 8), l16 + 1024, od);
  }
#undef WRT
#undef RDADD
  BARRIER();
  if (wid >= 4) {
    if (b >= 224) {
      asm volatile("s_waitcnt vmcnt(0)" ::: "memory");
      if (lane == 0 && lds_arrive(lds, 2) == 3) set_flag(ws, WS_F3 + (rb * 16 + h) * 128, tag);
    }
  } else if (b < 224) {
#pragma unroll
    for (int i = 0; i < 7; ++i) glds16((const char*)woB + (224 + i * 32) * 896, wo_vo, lds0 + L_CB + (i * 4 + wid) * 1024);
    if (wid == 0 && lane < 32) *(__attribute__((address_space(3))) u32x4*)((lds_ptr)lds + L_CB + 224 * 128 + lane * 16) = (u32x4){0u, 0u, 0u, 0u};
  }
  if (b >= 224) { STAMPDUMP(); return; }

  {
    const int rt = wid & 1, kq = wid >> 1;
    wait_flags(ws, lane < 4 ? WS_F3A + ((rg * 2 + rt) * 16 + kq * 4 + lane) * 128 : WS_FB + (14 * 16 + rg) * 128,
               lane < 5, tag);
    if (wid >= 4) {
      asm volatile("s_waitcnt vmcnt(0)" ::: "memory");
      if (lane == 0 && lds_arrive(lds, 2) == 3) { set_flag(ws, WS_F3 + (rb * 16 + h) * 128, tag); STAMPX(6); }
    }
    const float g = __uint_as_float(__builtin_amdgcn_raw_buffer_load_b32(rs, r32 * 4, WS_G + (rg * 2 + rt) * 128, 16));
    const unsigned oa = WS_OF + ((64 + rg * 2 + rt) * 16 + kq * 4) * 2048;
    bf16x8 of[8], og[8];
#pragma unroll
    for (int i = 0; i < 8; ++i) of[i] = ldg(rs, l16 + (i & 3) * 1024, oa + (i >> 2) * 4096);
    WAITBAR(9);
    __builtin_amdgcn_s_setprio(0);
    const int wlo = (4 * hh + q4) * 128 + cb * 32 + p4 * 8;
    const int sw = (q4 >> 1) * 64;
    lds_cptr wpb = (lds_cptr)lds + (kq < 2 ? L_WOB1 + kq * 4 * 28 * 128 : L_CB + (kq - 2) * 4 * 28 * 128) + wlo;
    bf16x8 wb[16];
#pragma unroll
    for (int i = 0; i < 8; ++i) {
      lds_cptr p = wpb + ((i >> 1) * 28 + 16 * (i & 1)) * 128;
      wb[2 * i] = cat8(trrd(p + sw), trrd(p + sw + 8 * 128));
      wb[2 * i + 1] = cat8(trrd(p + (64 - sw)), trrd(p + (64 - sw) + 8 * 128));
    }
    __builtin_amdgcn_sched_barrier(0);
    STAMP(7);
    wait_flags(ws, WS_F3 + ((rg >> 1) * 16 + kq * 4 + lane) * 128, lane < 4, tag);
    STAMP(10);
    const unsigned ob = WS_OF + ((rg * 2 + rt) * 16 + kq * 4) * 2048;
#pragma unroll
    for (int i = 0; i < 8; ++i) og[i] = ldg(rs, l16 + (i & 3) * 1024, ob + (i >> 2) * 4096);
    __builtin_amdgcn_sched_barrier(0);
    lds_cptr wpa = (lds_cptr)lds + L_WOA + kq * 4 * 28 * 128 + wlo;
    f32x16 a0 = zero, a1 = zero;
#pragma unroll
    for (int i = 0; i < 8; ++i) {
      lds_cptr p = wpa + ((i >> 1) * 28 + 16 * (i & 1)) * 128;
      const bf16x8 wf0 = cat8(trrd(p + sw), trrd(p + sw + 8 * 128));
      const bf16x8 wf1 = cat8(trrd(p + (64 - sw)), trrd(p + (64 - sw) + 8 * 128));
      a0 = MFMA(wf0, of[i], a0);
      a1 = MFMA(wf1, of[i], a1);
    }
#pragma unroll
    for (int r = 0; r < 16; ++r) { a0[r] *= g; a1[r] *= g; }
    BARRIER();
    f32x16 b0 = zero, b1 = zero;
#pragma unroll
    for (int i = 0; i < 8; ++i) {
      b0 = MFMA(wb[2 * i], og[i], b0);
      b1 = MFMA(wb[2 * i + 1], og[i], b1);
    }
    const float g1 = 1.f - g;
#pragma unroll
    for (int r = 0; r < 16; ++r) { a0[r] += g1 * b0[r]; a1[r] += g1 * b1[r]; }
    lds_ptr ab = (lds_ptr)lds + L_WOA + rt * 32768 + kq * 2048 + l16;
#define WRQ(dst, A, o8)                                                                 \
  {                                                                                     \
    lds_ptr p = ab + (dst) * 8192;                                                      \
    *(lf4)p = (f32x4){A[o8], A[o8 + 1], A[o8 + 2], A[o8 + 3]};                          \
    *(lf4)(p + 1024) = (f32x4){A[o8 + 4], A[o8 + 5], A[o8 + 6], A[o8 + 7]};             \
  }
    WRQ(0, a0, 0) WRQ(1, a0, 8) WRQ(2, a1, 0) WRQ(3, a1, 8)
#undef WRQ
    BARRIER();
    {
      lds_ptr p = (lds_ptr)lds + L_WOA + rt * 32768 + kq * 8192 + l16;
      f32x4 u = *(lf4)p, w = *(lf4)(p + 1024);
#pragma unroll
      for (int src = 1; src < 4; ++src) { u += *(lf4)(p + src * 2048); w += *(lf4)(p + src * 2048 + 1024); }
      const int row = rg * 64 + rt * 32 + r32;
      const int col = cg3 * 64 + (kq >> 1) * 32 + (kq & 1) * 16 + 4 * hh;
      *(u32x2*)(out + (size_t)row * 448 + col) = (u32x2){pk2(u[0], u[1]), pk2(u[2], u[3])};
      *(u32x2*)(out + (size_t)row * 448 + col + 8) = (u32x2){pk2(w[0], w[1]), pk2(w[2], w[3])};
    }
  }
  STAMP(8);
  STAMPDUMP();
}

extern "C" void kernel_launch(void* const* d_in, const int* in_sizes, int n_in, void* d_out, int out_size,
                              void* d_ws, size_t ws_size, hipStream_t stream) {
  const u16* x = (const u16*)d_in[0];
  const u16* wqkvA = (const u16*)d_in[1];
  const u16* woA = (const u16*)d_in[2];
  const u16* wqkvB = (const u16*)d_in[4];
  const u16* woB = (const u16*)d_in[5];
  const u16* gw = (const u16*)d_in[6];
  (void)hipFuncSetAttribute((const void*)fused_k, hipFuncAttributeMaxDynamicSharedMemorySize, LDS_BYTES);
  hipLaunchKernelGGL(fused_k, dim3(256), dim3(512), LDS_BYTES, stream, x, wqkvA, wqkvB, woA, woB, gw, (char*)d_ws, (u16*)d_out);
}
```

```cpp
#include <hip/hip_runtime.h>
#include <hip/hip_bf16.h>
#include <stdint.h>

typedef unsigned short u16;
typedef unsigned long long u64;
typedef short bf16x8 __attribute__((ext_vector_type(8)));
typedef short s16x4 __attribute__((ext_vector_type(4)));
typedef float f32x16 __attribute__((ext_vector_type(16)));
typedef float f32x2 __attribute__((ext_vector_type(2)));
typedef float f32x4 __attribute__((ext_vector_type(4)));
typedef unsigned u32x4 __attribute__((ext_vector_type(4)));
typedef unsigned u32x2 __attribute__((ext_vector_type(2)));
typedef __bf16 bf2_t __attribute__((ext_vector_type(2)));
typedef __attribute__((address_space(3))) const char* lds_cptr;
typedef __attribute__((address_space(3))) char* lds_ptr;
typedef __attribute__((address_space(1))) u64 gu64;
typedef short v4i16_t __attribute__((ext_vector_type(4)));

#define DI __device__ __forceinline__
#define MFMA(a, b, c) __builtin_amdgcn_mfma_f32_32x32x16_bf16((a), (b), (c), 0, 0, 0)
#define BARRIER() asm volatile("s_waitcnt lgkmcnt(0)\n\ts_barrier" ::: "memory")
#define WAITBAR(N) asm volatile("s_waitcnt vmcnt(" #N ") lgkmcnt(0)\n\ts_barrier" ::: "memory")

extern "C" __device__ u64 gm_dispatch_id() __asm("llvm.amdgcn.dispatch.id");

#define CODE_LINES 288
constexpr float QSCALE = 0.18898223650461362f * 1.4426950408889634f;

constexpr unsigned WS_QF = 0;
constexpr unsigned WS_KV = 4u << 20;
constexpr unsigned WS_OF = 12u << 20;
constexpr unsigned WS_G = 16u << 20;
constexpr unsigned WS_FA = WS_G + 65536;
constexpr unsigned WS_FB = WS_FA + 32768;
constexpr unsigned WS_F3 = WS_FB + 32768;
constexpr unsigned WS_F3A = WS_F3 + 32768;
constexpr unsigned WS_END = WS_F3A + 131072;

constexpr int LDS_BYTES = 163840;
constexpr int L_X = 0;
constexpr int L_WR = 98304;
constexpr int L_QX = 153600;
constexpr int L_CNT = 163328;
constexpr int L_CB = 0;
constexpr int L_WOA = 65536;
constexpr int L_WOB1 = 123392;

DI unsigned pk2(float a, float b) {
  f32x2 v = {a, b};
  bf2_t r = __builtin_convertvector(v, bf2_t);
  return __builtin_bit_cast(unsigned, r);
}
DI u32x4 pack8u(const f32x16& x, int o) {
  u32x4 p;
  p[0] = pk2(x[o + 0], x[o + 1]); p[1] = pk2(x[o + 2], x[o + 3]);
  p[2] = pk2(x[o + 4], x[o + 5]); p[3] = pk2(x[o + 6], x[o + 7]);
  return p;
}
DI bf16x8 pack8(const f32x16& x, int o) { return __builtin_bit_cast(bf16x8, pack8u(x, o)); }
DI void glds16(const void* sbase, unsigned voff, unsigned lds_base) {
  unsigned sv;
  asm volatile("s_mov_b32 %0, m0\n\ts_mov_b32 m0, %3\n\ts_nop 4\n\tglobal_load_lds_dwordx4 %1, %2\n\ts_mov_b32 m0, %0"
               : "=&s"(sv) : "v"(voff), "s"(sbase), "s"(lds_base) : "memory");
}
DI s16x4 trrd(lds_cptr p) {
  return __builtin_bit_cast(s16x4, __builtin_amdgcn_ds_read_tr16_b64_v4i16((__attribute__((address_space(3))) v4i16_t*)p));
}
DI bf16x8 cat8(s16x4 lo, s16x4 hi) { return __builtin_shufflevector(lo, hi, 0, 1, 2, 3, 4, 5, 6, 7); }
DI bf16x8 ldsrd16(lds_cptr p) { return *(const __attribute__((address_space(3))) bf16x8*)p; }
DI float xhalf_max(float m) {
  auto rr = __builtin_amdgcn_permlane32_swap(__float_as_uint(m), __float_as_uint(m), false, false);
  return fmaxf(__uint_as_float(rr[0]), __uint_as_float(rr[1]));
}
DI float xhalf_sum(float m) {
  auto rr = __builtin_amdgcn_permlane32_swap(__float_as_uint(m), __float_as_uint(m), false, false);
  return __uint_as_float(rr[0]) + __uint_as_float(rr[1]);
}
DI float max16(const f32x16& s) {
  float a = fmaxf(fmaxf(s[0], s[1]), s[2]), b = fmaxf(fmaxf(s[3], s[4]), s[5]);
  a = fmaxf(fmaxf(a, s[6]), s[7]); b = fmaxf(fmaxf(b, s[8]), s[9]);
  a = fmaxf(fmaxf(a, s[10]), s[11]); b = fmaxf(fmaxf(b, s[12]), s[13]);
  a = fmaxf(fmaxf(a, s[14]), s[15]);
  return fmaxf(a, b);
}
DI bf16x8 ldg(__amdgpu_buffer_rsrc_t rs, unsigned voff, unsigned soff) {
  return __builtin_bit_cast(bf16x8, __builtin_amdgcn_raw_buffer_load_b128(rs, voff, soff, 16));
}
DI void stg(__amdgpu_buffer_rsrc_t rs, u32x4 v, unsigned voff, unsigned soff) {
  __builtin_amdgcn_raw_buffer_store_b128(v, rs, voff, soff, 16);
}
DI void wait_flags(const char* ws, unsigned off, bool active, u64 tag) {
  gu64* p = (gu64*)(ws + off);
  const u64 t0 = __builtin_amdgcn_s_memrealtime();
  for (;;) {
    u64 v = tag;
    if (active) v = __hip_atomic_load(p, __ATOMIC_RELAXED, __HIP_MEMORY_SCOPE_AGENT);
    if (__all(v == tag)) break;
    __builtin_amdgcn_s_sleep(1);
    if (__builtin_amdgcn_s_memrealtime() - t0 > 5000000ull) break;
  }
  __builtin_amdgcn_fence(__ATOMIC_ACQUIRE, "wavefront");
}
DI void set_flag(char* ws, unsigned off, u64 tag) {
  __hip_atomic_store((gu64*)(ws + off), tag, __ATOMIC_RELAXED, __HIP_MEMORY_SCOPE_AGENT);
}

DI void poll_fa(const char* ws, int lane, int wid, int h, int rb, u64 tag) {
  const int cw = (lane & 1) ? (28 * h + 27) >> 6 : (28 * h) >> 6;
  const int kind = lane >> 1;
  const int g = kind == 5 ? cw >> 1 : (kind == 2 || kind == 3) ? 7 + cw : (7 + cw) >> 1;
  const int r = kind == 5 ? rb : kind == 4 ? 0 : 2 * wid + (kind & 1);
  wait_flags(ws, WS_FA + (g * 16 + r) * 128, lane < (wid < 4 ? 12 : 8), tag);
}

typedef __attribute__((address_space(3))) unsigned* lds_u32p;
DI unsigned lds_arrive(char* lds, int idx) {
  return __hip_atomic_fetch_add((lds_u32p)((lds_ptr)lds + L_CNT) + idx, 1u, __ATOMIC_RELAXED, __HIP_MEMORY_SCOPE_WORKGROUP);
}
DI void lds_wait(char* lds, int idx, unsigned n) {
  while (__hip_atomic_load((lds_u32p)((lds_ptr)lds + L_CNT) + idx, __ATOMIC_RELAXED, __HIP_MEMORY_SCOPE_WORKGROUP) < n) __builtin_amdgcn_s_sleep(1);
  asm volatile("" ::: "memory");
}

struct P1Ctx {
  const char* x; const char* w1; const char* w2; const char* w3;
  unsigned lds0, xoff0, xoff1, woff;
  int wid, lane, rot;
};
DI int rotc(const P1Ctx& c, int ch) { const int t = ch + c.rot; return t >= 7 ? t - 7 : t; }
DI void p1_x(const P1Ctx& c, int ch) {
  const int slot = ch == 6 ? 0 : ch;
  const char* src = c.x + rotc(c, ch) * 128;
  glds16(src, c.xoff0, c.lds0 + L_X + slot * 16384 + c.wid * 1024);
  glds16(src, c.xoff1, c.lds0 + L_X + slot * 16384 + (c.wid + 8) * 1024);
}
DI void p1_w12(const P1Ctx& c, int ch) {
  const int slot = ch % 3;
  const int ro = rotc(c, ch) * (64 * 2688);
  glds16(c.w1 + ro, c.woff, c.lds0 + L_WR + slot * 16384 + c.wid * 1024);
  glds16(c.w2 + ro, c.woff, c.lds0 + L_WR + slot * 16384 + 8192 + c.wid * 1024);
}
DI constexpr int w3_off(int ch) { return L_X + (1 + (ch >> 1)) * 16384 + (ch & 1) * 8192; }
DI void p1_w3(const P1Ctx& c, int ch) {
  glds16(c.w3 + rotc(c, ch) * (64 * 2688), c.woff, c.lds0 + w3_off(ch) + c.wid * 1024);
}

template <bool ISV>
DI void p1_store(const f32x16& a, int br, int w, int ct, int t64, int lane, __amdgpu_buffer_rsrc_t rs) {
  const int r32 = lane & 31, hh = lane >> 5;
  const int sec = w / 7, wc = (w - sec * 7) * 64;
  if (ISV) {
    const int c = wc + ct * 32 + r32, head = c / 28, d = c - head * 28;
    const unsigned off = WS_KV + ((br * 16 + head) * 64 + t64) * 4096 + 2048 + (hh * 32 + d) * 16;
    stg(rs, pack8u(a, 0), off, 0);
    stg(rs, pack8u(a, 8), off + 1024, 0);
    if (d >= 24) {
      const unsigned f = d == 24 ? 0x3F803F80u : 0u;
      const u32x4 pv = {f, f, f, f};
      stg(rs, pv, off + 64, 0);
      stg(rs, pv, off + 64 + 1024, 0);
    }
  } else {
    const float sc = sec == 0 ? QSCALE : 1.f;
    unsigned w8[8];
#pragma unroll
    for (int i = 0; i < 8; ++i) w8[i] = pk2(a[2 * i] * sc, a[2 * i + 1] * sc);
    const unsigned padv = sec == 0 ? 0u : 0x3F80u;
    const unsigned hstride = sec == 0 ? 64u * 2048u : 64u * 4096u;
    const unsigned base0 = (sec == 0 ? WS_QF + (br * 16 * 64 + t64) * 2048 : WS_KV + (br * 16 * 64 + t64) * 4096) + r32 * 16;
#pragma unroll
    for (int pp = 0; pp < 2; ++pp) {
      const auto s0 = __builtin_amdgcn_permlane32_swap(w8[4 * pp], w8[4 * pp + 2], false, false);
      const auto s1 = __builtin_amdgcn_permlane32_swap(w8[4 * pp + 1], w8[4 * pp + 3], false, false);
      const int C = (wc >> 3) + ct * 4 + 2 * pp + hh, hp = C / 7, r = C - hp * 7;
      const int head = 2 * hp + (r > 3 ? 1 : 0), cc = r > 3 ? r - 3 : r;
      u32x4 v = {s0[0], s1[0], s0[1], s1[1]};
      if (r == 3) {
        const u32x4 v2 = {padv, 0u, s0[1], s1[1]};
        stg(rs, v2, base0 + (head + 1) * hstride, 0);
        v[2] = padv; v[3] = 0u;
      }
      stg(rs, v, base0 + head * hstride + (cc >> 1) * 1024 + (cc & 1) * 512, 0);
    }
  }
}

template <bool V1, bool V2>
DI void p1_sweep1(const P1Ctx& c, char* lds, bf16x8 (&xf)[28], f32x16& acc0, f32x16& acc1, int wl) {
  const int lane = c.lane, r32 = lane & 31, hh = lane >> 5;
  const int xrow = (c.wid & 3) * 32 + r32, xsw = (xrow >> 1) & 7;
  lds_cptr xb = (lds_cptr)lds + L_X + xrow * 128;
  bf16x8 wf[2][8];
#pragma unroll
  for (int ch = 0; ch < 8; ++ch) {
    if (ch == 7) __builtin_amdgcn_sched_barrier(0);
    else if (ch == 0) WAITBAR(8);
    else if (ch <= 4) WAITBAR(6);
    else if (ch == 5) WAITBAR(4);
    else if (ch == 6) WAITBAR(4);
    lds_cptr wb = (lds_cptr)lds + L_WR + (ch % 3) * 16384 + wl;
    lds_cptr xc = xb + (ch == 6 ? 0 : ch) * 16384;
#pragma unroll
    for (int s = 0; s < 4; ++s) {
      if (ch < 7) {
        xf[ch * 4 + s] = ldsrd16(xc + (((2 * s + hh) ^ xsw) * 16));
        lds_cptr wp = wb + s * 2048;
        wf[ch & 1][s] = cat8(trrd(wp), trrd(wp + 512));
        wf[ch & 1][4 + s] = cat8(trrd(wp + 8192), trrd(wp + 8192 + 512));
      }
      if (ch >= 1) {
        const bf16x8 x8 = xf[(ch - 1) * 4 + s];
        const bf16x8 w0 = wf[(ch - 1) & 1][s], w1 = wf[(ch - 1) & 1][4 + s];
        acc0 = V1 ? MFMA(x8, w0, acc0) : MFMA(w0, x8, acc0);
        acc1 = V2 ? MFMA(x8, w1, acc1) : MFMA(w1, x8, acc1);
      }
      __builtin_amdgcn_sched_barrier(0);
      if (s == 0 && ch >= 1 && ch <= 4) p1_w12(c, ch + 2);
      if (s == 1 && ch <= 3) p1_x(c, ch + 3);
      if (s == 0 && ch == 5) { p1_w3(c, 0); p1_w3(c, 1); }
      if (s == 1 && ch == 5) { p1_w3(c, 2); p1_w3(c, 3); }
      if (s == 0 && ch == 6) { p1_w3(c, 4); p1_w3(c, 5); }
      if (s == 1 && ch == 6) { p1_w3(c, 6); }
      __builtin_amdgcn_sched_barrier(0);
    }
  }
}
template <bool V3, int C0, int C1>
DI void p1_sweep2(char* lds, const bf16x8 (&xf)[28], f32x16& acc0, int wl) {
#pragma unroll
  for (int ch = C0; ch < C1; ++ch) {
    lds_cptr wb = (lds_cptr)lds + w3_off(ch) + wl;
#pragma unroll
    for (int s = 0; s < 4; ++s) {
      lds_cptr wp = wb + s * 2048;
      const bf16x8 w0 = cat8(trrd(wp), trrd(wp + 512));
      acc0 = V3 ? MFMA(xf[ch * 4 + s], w0, acc0) : MFMA(w0, xf[ch * 4 + s], acc0);
    }
  }
}

__global__ __launch_bounds__(512, 1) void fused_k(const u16* __restrict__ x, const u16* __restrict__ wA,
                                                  const u16* __restrict__ wB, const u16* __restrict__ woA,
                                                  const u16* __restrict__ woB, const u16* __restrict__ gw,
                                                  char* ws, u16* __restrict__ out) {
  extern __shared__ __attribute__((aligned(16))) char lds[];
  unsigned code_touch = 0;
  {
    u64 pc;
    asm volatile("s_getpc_b64 %0" : "=s"(pc));
    if (blockIdx.x < 40 && threadIdx.x >= 448)
      code_touch = *(const __attribute__((address_space(1))) unsigned*)((pc & ~127ull) + (u64)min((blockIdx.x >> 3) * 64 + (threadIdx.x & 63), CODE_LINES - 1) * 128);
  }
  asm volatile("" :: "s"(x), "s"(wA), "s"(wB), "s"(woA), "s"(woB), "s"(gw), "s"(ws), "s"(out));
  const int tid = threadIdx.x, lane = tid & 63;
  const int wid = __builtin_amdgcn_readfirstlane(tid >> 6);
  const int b = blockIdx.x;
  const int r32 = lane & 31, hh = lane >> 5;
  const int g16 = lane >> 4, i16 = lane & 15, q4 = i16 >> 2, p4 = i16 & 3, cb = g16 & 1;
  const unsigned lds0 = (unsigned)(uintptr_t)lds;
  const __amdgpu_buffer_rsrc_t rs = __builtin_amdgcn_make_buffer_rsrc((void*)ws, 0, (int)WS_END, 0x00020000);
  const u64 tag = ((gm_dispatch_id() + 1) * 0x9E3779B97F4A7C15ull) ^ (u64)(uintptr_t)__builtin_amdgcn_dispatch_ptr() ^ 0x5A17C3E9D2B40F61ull;
  const f32x16 zero = {};
  const unsigned l16 = lane * 16;
  const int h = (b & 7) + 8 * ((b >> 3) & 1), rb = b >> 4;
  unsigned fb_off = 0;
  f32x16 dA0 = zero, dA1 = zero;
  int d_w3 = 0, d_w2 = 0, d_n = 0, d_t64 = 0;
  if (tid < 8) ((__attribute__((address_space(3))) unsigned*)((lds_ptr)lds + L_CNT))[tid] = 0u;

  if (__builtin_expect(b < 224, 1)) {
    const int i5 = b >> 3, g = (b & 1) + 2 * (i5 % 7), rb1 = 4 * ((b & 7) >> 1) + i5 / 7;
    const int w1 = g < 7 ? 2 * g : 7 + g, w2 = g < 7 ? 2 * g + 1 : 2 * g - 7, w3 = g < 7 ? g : 2 * g - 6;
    const int br2 = g < 7 ? 0 : 1;
    P1Ctx c;
    c.x = (const char*)x; c.lds0 = lds0; c.wid = wid; c.lane = lane;
    c.rot = (i5 % 7 + 2 * (i5 / 7)) % 7;
    c.w1 = (const char*)wB + w1 * 128;
    c.w2 = (const char*)(g < 7 ? wB : wA) + w2 * 128;
    c.w3 = (const char*)wA + w3 * 128;
    {
      const int rl8 = lane >> 3, row0 = 8 * wid + rl8, row1 = row0 + 64;
      c.xoff0 = (unsigned)((rb1 * 128 + row0) * 896 + (((lane & 7) ^ ((row0 >> 1) & 7)) * 16));
      c.xoff1 = (unsigned)((rb1 * 128 + row1) * 896 + (((lane & 7) ^ ((row1 >> 1) & 7)) * 16));
      c.woff = (unsigned)(row0 * 2688 + (((lane & 7) ^ (((rl8 >> 1) & 1) << 2)) * 16));
    }
    p1_x(c, 0); p1_w12(c, 0);
    asm volatile("s_barrier" ::: "memory");
    p1_x(c, 1); p1_w12(c, 1); p1_x(c, 2); p1_w12(c, 2);
    const int rt = wid & 3, ct = wid >> 2, t64 = rb1 * 4 + rt;
    const int wl = (8 * hh + q4) * 128 + ((ct ^ (q4 >> 1)) * 64 + cb * 32 + p4 * 8);
    bf16x8 xf[28];
    f32x16 acc0 = zero, acc1 = zero;
    if (g < 7) p1_sweep1<false, false>(c, lds, xf, acc0, acc1, wl);
    else if (g < 11) p1_sweep1<true, false>(c, lds, xf, acc0, acc1, wl);
    else p1_sweep1<true, true>(c, lds, xf, acc0, acc1, wl);
    if (g < 7) {
      f32x16 cur = acc0;
#pragma unroll 1
      for (int it = 0; it < 2; ++it) { p1_store<false>(cur, 0, w1 + it, ct, t64, lane, rs); cur = acc1; }
    } else {
      p1_store<true>(acc0, 0, w1, ct, t64, lane, rs);
    }
    WAITBAR(4);
    acc0 = zero;
    if (g < 10) p1_sweep2<false, 0, 2>(lds, xf, acc0, wl); else p1_sweep2<true, 0, 2>(lds, xf, acc0, wl);
    asm volatile("s_waitcnt vmcnt(0)" ::: "memory");
    if (lane == 0) {
      const unsigned old = __hip_atomic_fetch_add((__attribute__((address_space(3))) unsigned*)((lds_ptr)lds + L_CNT + 4), 1u, __ATOMIC_RELAXED, __HIP_MEMORY_SCOPE_WORKGROUP);
      if (old == 7) { set_flag(ws, WS_FA + (g * 16 + rb1) * 128, tag); }
    }
    if (g < 10) p1_sweep2<false, 2, 7>(lds, xf, acc0, wl); else p1_sweep2<true, 2, 7>(lds, xf, acc0, wl);
    dA0 = acc0; dA1 = acc1; d_w3 = w3; d_w2 = w2; d_n = 1 + br2; d_t64 = t64;
    fb_off = WS_FB + (g * 16 + rb1) * 128;
  } else {
    typedef const __attribute__((address_space(1))) unsigned* gptr;
    const int q = (b - 224) >> 3;
    const int l0 = q * 784 + tid, l1 = q * 784 + min(tid + 512, 783);
    const unsigned t1 = *(gptr)((u64)woB + (u64)l0 * 128), t2 = *(gptr)((u64)woB + (u64)l1 * 128);
    const unsigned t3 = *(gptr)((u64)woA + (u64)l0 * 128), t4 = *(gptr)((u64)woA + (u64)l1 * 128);
    const unsigned acc = t1 ^ t2 ^ t3 ^ t4;
    asm volatile("" :: "v"(acc));
    {
      const int row0 = (b - 224) * 64 + wid * 8;
      const int lc = min(lane, 55);
      const u32x4 gv = *(const __attribute__((address_space(1))) u32x4*)((u64)gw + lc * 16);
      u32x4 xv[8];
#pragma unroll
      for (int i = 0; i < 8; ++i) xv[i] = *(const __attribute__((address_space(1))) u32x4*)((u64)x + (u64)(row0 + i) * 896 + lc * 16);
      float res = 0.f;
#pragma unroll
      for (int i = 0; i < 8; ++i) {
        float z = 0.f;
#pragma unroll
        for (int e = 0; e < 4; ++e) {
          const unsigned xe = xv[i][e], ge = gv[e];
          z = __builtin_amdgcn_fdot2_f32_bf16(__builtin_bit_cast(bf2_t, xe), __builtin_bit_cast(bf2_t, ge), z, false);
        }
        if (lane >= 56) z = 0.f;
#pragma unroll
        for (int o = 32; o >= 1; o >>= 1) z += __shfl_xor(z, o);
        if (lane == i) res = 1.0f / (1.0f + __expf(-z));
      }
      if (lane < 8) __builtin_amdgcn_raw_buffer_store_b32(__float_as_uint(res), rs, lane * 4, WS_G + row0 * 4, 16);
      WAITBAR(0);
      if (tid == 0) set_flag(ws, WS_FB + (14 * 16 + b - 224) * 128, tag);
    }
  }

  asm volatile("" :: "v"(code_touch));
  BARRIER();
  poll_fa(ws, lane, wid, h, rb, tag);
  typedef __attribute__((address_space(3))) f32x4* lf4;
  const int i5 = b >> 3, rg = (b & 7) + 8 * (i5 / 7), cg3 = i5 % 7;
  const unsigned wo_vo = (unsigned)(((wid & 3) * 8 + (lane >> 3)) * 896 + cg3 * 128 + (((lane & 7) ^ (((lane >> 4) & 1) << 2)) * 16));
#define WRT(dst, O)                                                                     \
  {                                                                                     \
    lds_ptr p_ = (lds_ptr)lds + L_CB + (dst) * 16384 + (wid & 3) * 4096 + l16;          \
    *(lf4)p_ = (f32x4){O[0], O[1], O[2], O[3]};                                         \
    *(lf4)(p_ + 1024) = (f32x4){O[4], O[5], O[6], O[7]};                                \
    *(lf4)(p_ + 2048) = (f32x4){O[8], O[9], O[10], O[11]};                              \
    *(lf4)(p_ + 3072) = (f32x4){O[12], O[13], O[14], O[15]};                            \
  }
  f32x16 o0 = zero, o1 = zero, o2 = zero, o3 = zero;
  {
    const unsigned kvh = WS_KV + (h * 64) * 4096;
    bf16x8 qw0, qw1, kr0, kr1;
    if (wid < 4) {
      const unsigned qb = WS_QF + (h * 64 + rb * 4 + wid) * 2048;
      qw0 = ldg(rs, l16, qb); qw1 = ldg(rs, l16 + 1024, qb);
      kr0 = ldg(rs, l16, kvh); kr1 = ldg(rs, l16 + 1024, kvh);
    }
    const unsigned kvw = kvh + wid * 8 * 4096;
#define TADDR(t) (kvw + (unsigned)((((t) + rb) & 7) * 4096))
    bf16x8 ka0 = ldg(rs, l16, TADDR(0)), ka1 = ldg(rs, l16 + 1024, TADDR(0));
    bf16x8 kb0 = ldg(rs, l16, TADDR(1)), kb1 = ldg(rs, l16 + 1024, TADDR(1));
    bf16x8 va0 = ldg(rs, l16 + 2048, TADDR(0)), va1 = ldg(rs, l16 + 3072, TADDR(0));
    bf16x8 vc0, vc1;
    if (wid < 4) {
      const bool odd = h & 1;
      f32x16 sr = MFMA(kr0, qw0, zero); sr = MFMA(kr1, qw1, sr);
      const unsigned nm = pk2(-xhalf_max(max16(sr)), 0.f);
      u32x4 ta = __builtin_bit_cast(u32x4, qw0), tb = __builtin_bit_cast(u32x4, qw1);
      if (odd ? !hh : hh) { if (odd) ta[0] = nm; else tb[2] = nm; }
      *(__attribute__((address_space(3))) u32x4*)((lds_ptr)lds + L_QX + wid * 2048 + l16) = ta;
      *(__attribute__((address_space(3))) u32x4*)((lds_ptr)lds + L_QX + wid * 2048 + 1024 + l16) = tb;
      asm volatile("s_waitcnt lgkmcnt(0)" ::: "memory");
      if (lane == 0) lds_arrive(lds, 5);
    }
    lds_wait(lds, 5, 4);
    bf16x8 kc0 = ldg(rs, l16, TADDR(2)), kc1 = ldg(rs, l16 + 1024, TADDR(2));
    bf16x8 vb0 = ldg(rs, l16 + 2048, TADDR(1)), vb1 = ldg(rs, l16 + 3072, TADDR(1));
    lds_cptr qx = (lds_cptr)lds + L_QX + l16;
    const bf16x8 q00 = ldsrd16(qx), q01 = ldsrd16(qx + 1024), q10 = ldsrd16(qx + 2048), q11 = ldsrd16(qx + 3072);
    const bf16x8 q20 = ldsrd16(qx + 4096), q21 = ldsrd16(qx + 5120), q30 = ldsrd16(qx + 6144), q31 = ldsrd16(qx + 7168);
    if (wid >= 4) __builtin_amdgcn_s_setprio(1);
    f32x16 s0 = MFMA(ka0, q00, zero); s0 = MFMA(ka1, q01, s0);
    f32x16 s1 = MFMA(ka0, q10, zero); s1 = MFMA(ka1, q11, s1);
    f32x16 s2 = MFMA(ka0, q20, zero); s2 = MFMA(ka1, q21, s2);
    f32x16 s3 = zero;
    u32x4 pa, pb0, pb1, pb2, pb3;
    {
      f32x16 cur = dA0;
      int wsel = d_w3;
#pragma unroll 1
      for (int it = 0; it < d_n; ++it) {
        if (wsel >= 14) p1_store<true>(cur, 1, wsel, wid >> 2, d_t64, lane, rs); else p1_store<false>(cur, 1, wsel, wid >> 2, d_t64, lane, rs);
        cur = dA1; wsel = d_w2;
      }
    }
    auto wo_prefetch = [&]() {
      const int hi4 = wid >> 2;
#pragma unroll
      for (int i = 0; i < 7; ++i) glds16((const char*)woA + (2 * i + hi4) * 32 * 896, wo_vo, lds0 + L_WOA + ((2 * i + hi4) * 4 + (wid & 3)) * 1024);
#pragma unroll
      for (int i = 0; i < 4; ++i)
        if (2 * i + hi4 < 7 || wid == 4) glds16((const char*)woB + (2 * i + hi4) * 32 * 896, wo_vo, lds0 + L_WOB1 + ((2 * i + hi4) * 4 + (wid & 3)) * 1024);
      if (wid == 5 && lane < 32) *(__attribute__((address_space(3))) u32x4*)((lds_ptr)lds + L_WOA + 448 * 128 + lane * 16) = (u32x4){0u, 0u, 0u, 0u};
    };
#define SB() __builtin_amdgcn_sched_barrier(0)
#define PIN(x) asm volatile("" : "+v"(x))
#define EX4(S, i) { S[i] = __builtin_amdgcn_exp2f(S[i]); S[i + 1] = __builtin_amdgcn_exp2f(S[i + 1]); S[i + 2] = __builtin_amdgcn_exp2f(S[i + 2]); S[i + 3] = __builtin_amdgcn_exp2f(S[i + 3]); }
#define BLK(S, O, PB, V0, PS, PO, PPB, PV1, PK0, PK1, PQA, PQB, DOPV, DOQK)              \
  {                                                                                      \
    if (DOPV) PO = MFMA(PV1, __builtin_bit_cast(bf16x8, PPB), PO);                       \
    EX4(S, 0) PIN(S); SB();                                                              \
    if (DOQK) PS = MFMA(PK0, PQA, zero);                                                 \
    EX4(S, 4) pa = pack8u(S, 0); PIN(S); PIN(pa); SB();                                  \
    if (DOQK) PS = MFMA(PK1, PQB, PS);                                                   \
    EX4(S, 8) PIN(S); SB();                                                              \
    O = MFMA(V0, __builtin_bit_cast(bf16x8, pa), O);                                     \
    EX4(S, 12) PB = pack8u(S, 8); PIN(PB); SB();                                         \
  }
#define STEP(t, V0, V1, K0, K1, PV0, PV1, PK0, PK1)                                      \
  {                                                                                      \
    BLK(s0, o0, pb0, V0, s3, o3, pb3, PV1, PK0, PK1, q30, q31, t > 0, true)              \
    if (t == 1 && fb_off) {   \
      asm volatile("s_waitcnt vmcnt(4)" ::: "memory");                                   \
      if (lane == 0) {                                                                   \
        const unsigned old = __hip_atomic_fetch_add((__attribute__((address_space(3))) unsigned*)((lds_ptr)lds + L_CNT), 1u, __ATOMIC_RELAXED, __HIP_MEMORY_SCOPE_WORKGROUP); \
        if (old == 7) set_flag(ws, fb_off, tag);                                         \
      }                                                                                  \
      SB();                                                                              \
    }                                                                                    \
    if (t + 3 < 8) { PK0 = ldg(rs, l16, TADDR(t + 3)); PK1 = ldg(rs, l16 + 1024, TADDR(t + 3)); }        \
    if (t + 2 < 8) { PV0 = ldg(rs, l16 + 2048, TADDR(t + 2)); PV1 = ldg(rs, l16 + 3072, TADDR(t + 2)); } \
    if (t == 1 && fb_off) wo_prefetch();                                                 \
    SB();                                                                                \
    if (t == 7 && wid < 4) { lds_wait(lds, 4, 4); SB(); }                                \
    BLK(s1, o1, pb1, V0, s0, o0, pb0, V1, K0, K1, q00, q01, true, t < 7)                 \
    if (t == 7) { WRT(0, o0) SB(); }                                                     \
    BLK(s2, o2, pb2, V0, s1, o1, pb1, V1, K0, K1, q10, q11, true, t < 7)                 \
    if (t == 7) { WRT(1, o1) SB(); }                                                     \
    BLK(s3, o3, pb3, V0, s2, o2, pb2, V1, K0, K1, q20, q21, true, t < 7)                 \
    if (t == 7) { WRT(2, o2) SB(); }                                                     \
  }
    STEP(0, va0, va1, kb0, kb1, vc0, vc1, ka0, ka1)
    STEP(1, vb0, vb1, kc0, kc1, va0, va1, kb0, kb1)
    STEP(2, vc0, vc1, ka0, ka1, vb0, vb1, kc0, kc1)
    STEP(3, va0, va1, kb0, kb1, vc0, vc1, ka0, ka1)
    STEP(4, vb0, vb1, kc0, kc1, va0, va1, kb0, kb1)
    STEP(5, vc0, vc1, ka0, ka1, vb0, vb1, kc0, kc1)
    STEP(6, va0, va1, kb0, kb1, vc0, vc1, ka0, ka1)
    STEP(7, vb0, vb1, kc0, kc1, va0, va1, kb0, kb1)
    o3 = MFMA(vb1, __builtin_bit_cast(bf16x8, pb3), o3);
    WRT(3, o3)
#undef TADDR
#undef BLK
#undef EX4
#undef STEP
  }
#define RDADD(ACC)                                                                      \
  {                                                                                     \
    lds_ptr p_ = (lds_ptr)lds + L_CB + (wid & 3) * 16384 + l16;                         \
    _Pragma("unroll") for (int src = 0; src < 4; ++src)                                 \
      _Pragma("unroll") for (int q_ = 0; q_ < 4; ++q_) {                                \
        const f32x4 t_ = *(lf4)(p_ + src * 4096 + q_ * 1024);                           \
        ACC[4 * q_] += t_[0]; ACC[4 * q_ + 1] += t_[1]; ACC[4 * q_ + 2] += t_[2]; ACC[4 * q_ + 3] += t_[3]; \
      }                                                                                 \
  }
  f32x16 oh = zero;
  if (wid >= 4) {
    asm volatile("s_waitcnt lgkmcnt(0)" ::: "memory");
    if (lane == 0) lds_arrive(lds, 3);
    {
      const int pr = rb > 0 ? rb - 1 : rb;
      const int cw = (lane & 1) ? (28 * h + 27) >> 6 : (28 * h) >> 6;
      const int kind = lane >> 1;
      const int gg = kind == 0 ? cw : (kind & 1) ? 7 + (cw >> 1) : 7 + ((7 + cw) >> 1);
      wait_flags(ws, WS_FB + (gg * 16 + (kind >= 3 ? pr : rb)) * 128, lane < 10, tag);
    }
    const int t64 = rb * 4 + wid - 4;
    const unsigned qo = WS_QF + ((16 + h) * 64 + t64) * 2048;
    const unsigned kc = WS_KV + ((16 + h) * 64 + t64) * 4096;
    const unsigned kp = t64 > 0 ? kc - 4096 : kc;
    const bf16x8 q0 = ldg(rs, l16, qo), q1 = ldg(rs, l16 + 1024, qo);
    const bf16x8 kc0 = ldg(rs, l16, kc), kc1 = ldg(rs, l16 + 1024, kc), vc0 = ldg(rs, l16 + 2048, kc), vc1 = ldg(rs, l16 + 3072, kc);
    const bf16x8 kp0 = ldg(rs, l16, kp), kp1 = ldg(rs, l16 + 1024, kp), vp1 = ldg(rs, l16 + 3072, kp);
    lds_wait(lds, 3, 4);
    RDADD(oh)
    asm volatile("s_waitcnt lgkmcnt(0)" ::: "memory");
    if (lane == 0) lds_arrive(lds, 4);
    f32x16 sc = MFMA(kc0, q0, zero); sc = MFMA(kc1, q1, sc);
    f32x16 sp = MFMA(kp0, q0, zero); sp = MFMA(kp1, q1, sp);
    const u64 mk = (0x5555ull << (r32 + 18)) >> (4 * hh);
    const int mcur = (int)(unsigned)(mk >> 32), mprev = t64 > 0 ? (int)(unsigned)mk : 0;
#pragma unroll
    for (int r = 0; r < 16; ++r) {
      const int i = (r & 3) + 8 * (r >> 2);
      const unsigned selc = (unsigned)__builtin_amdgcn_sbfe(mcur, i, 1);
      sc[r] = __uint_as_float((__float_as_uint(sc[r]) & selc) | (0xFF800000u & ~selc));
      if (r >= 8) {
        const unsigned selp = (unsigned)__builtin_amdgcn_sbfe(mprev, i, 1);
        sp[r] = __uint_as_float((__float_as_uint(sp[r]) & selp) | (0xFF800000u & ~selp));
      }
    }
    float mx = max16(sc);
#pragma unroll
    for (int r = 8; r < 16; r += 2) mx = fmaxf(fmaxf(mx, sp[r]), sp[r + 1]);
    const float m = xhalf_max(mx);
#pragma unroll
    for (int r = 0; r < 16; ++r) {
      sc[r] = __builtin_amdgcn_exp2f(sc[r] - m);
      if (r >= 8) sp[r] = __builtin_amdgcn_exp2f(sp[r] - m);
    }
    f32x16 o = MFMA(vc0, pack8(sc, 0), zero);
    o = MFMA(vc1, pack8(sc, 8), o);
    o = MFMA(vp1, pack8(sp, 8), o);
    const float l = __shfl(o[12], 32 + r32);
    const float rl = 1.0f / l;
#pragma unroll
    for (int r = 0; r < 16; ++r) o[r] *= rl;
    if (hh) { o[12] = 0.f; o[13] = 0.f; o[14] = 0.f; o[15] = 0.f; }
    const unsigned od = WS_OF + ((64 + t64) * 16 + h) * 2048;
    stg(rs, pack8u(o, 0), l16, od);
    stg(rs, pack8u(o, 8), l16 + 1024, od);
    asm volatile("s_waitcnt vmcnt(0)" ::: "memory");
    if (lane == 0) set_flag(ws, WS_F3A + (t64 * 16 + h) * 128, tag);
  }
  BARRIER();
  if (wid >= 4) {
    RDADD(oh)
    float l = __shfl(oh[12], 32 + r32);
    const int t64 = rb * 4 + wid - 4;
    if (__any(!(l < 1.0e18f && l > 1.0e-18f))) {
      const unsigned qo = WS_QF + (h * 64 + t64) * 2048;
      const bf16x8 q0 = ldg(rs, l16, qo), q1 = ldg(rs, l16 + 1024, qo);
      float m = -INFINITY;
      oh = zero;
#pragma unroll 1
      for (int t = 0; t < 64; ++t) {
        const unsigned kv = WS_KV + (h * 64 + t) * 4096;
        const bf16x8 k0 = ldg(rs, l16, kv), k1 = ldg(rs, l16 + 1024, kv), v0 = ldg(rs, l16 + 2048, kv), v1 = ldg(rs, l16 + 3072, kv);
        f32x16 sx = MFMA(k0, q0, zero); sx = MFMA(k1, q1, sx);
        const float mn = fmaxf(m, xhalf_max(max16(sx)));
        const float al = __builtin_amdgcn_exp2f(m - mn);
        m = mn;
#pragma unroll
        for (int r = 0; r < 16; ++r) { oh[r] *= al; sx[r] = __builtin_amdgcn_exp2f(sx[r] - mn); }
        oh = MFMA(v0, pack8(sx, 0), oh);
        oh = MFMA(v1, pack8(sx, 8), oh);
      }
      l = __shfl(oh[12], 32 + r32);
    }
    const float rl = 1.0f / l;
#pragma unroll
    for (int r = 0; r < 16; ++r) oh[r] *= rl;
    if (hh) { oh[12] = 0.f; oh[13] = 0.f; oh[14] = 0.f; oh[15] = 0.f; }
    const unsigned od = WS_OF + (t64 * 16 + h) * 2048;
    stg(rs, pack8u(oh, 0), l16, od);
    stg(rs, pack8u(oh, 8), l16 + 1024, od);
  }
#undef WRT
#undef RDADD
  BARRIER();
  if (wid >= 4) {
    if (b >= 224) {
      asm volatile("s_waitcnt vmcnt(0)" ::: "memory");
      if (lane == 0 && lds_arrive(lds, 2) == 3) set_flag(ws, WS_F3 + (rb * 16 + h) * 128, tag);
    }
  } else if (b < 224) {
#pragma unroll
    for (int i = 0; i < 7; ++i) glds16((const char*)woB + (224 + i * 32) * 896, wo_vo, lds0 + L_CB + (i * 4 + wid) * 1024);
    if (wid == 0 && lane < 32) *(__attribute__((address_space(3))) u32x4*)((lds_ptr)lds + L_CB + 224 * 128 + lane * 16) = (u32x4){0u, 0u, 0u, 0u};
  }
  if (b >= 224) return;

  {
    const int rt = wid & 1, kq = wid >> 1;
    wait_flags(ws, lane < 4 ? WS_F3A + ((rg * 2 + rt) * 16 + kq * 4 + lane) * 128 : WS_FB + (14 * 16 + rg) * 128,
               lane < 5, tag);
    if (wid >= 4) {
      asm volatile("s_waitcnt vmcnt(0)" ::: "memory");
      if (lane == 0 && lds_arrive(lds, 2) == 3) { set_flag(ws, WS_F3 + (rb * 16 + h) * 128, tag); }
    }
    const float g = __uint_as_float(__builtin_amdgcn_raw_buffer_load_b32(rs, r32 * 4, WS_G + (rg * 2 + rt) * 128, 16));
    const unsigned oa = WS_OF + ((64 + rg * 2 + rt) * 16 + kq * 4) * 2048;
    bf16x8 of[8], og[8];
#pragma unroll
    for (int i = 0; i < 8; ++i) of[i] = ldg(rs, l16 + (i & 3) * 1024, oa + (i >> 2) * 4096);
    WAITBAR(9);
    __builtin_amdgcn_s_setprio(0);
    const int wlo = (4 * hh + q4) * 128 + cb * 32 + p4 * 8;
    const int sw = (q4 >> 1) * 64;
    lds_cptr wpb = (lds_cptr)lds + (kq < 2 ? L_WOB1 + kq * 4 * 28 * 128 : L_CB + (kq - 2) * 4 * 28 * 128) + wlo;
    bf16x8 wb[16];
#pragma unroll
    for (int i = 0; i < 8; ++i) {
      lds_cptr p = wpb + ((i >> 1) * 28 + 16 * (i & 1)) * 128;
      wb[2 * i] = cat8(trrd(p + sw), trrd(p + sw + 8 * 128));
      wb[2 * i + 1] = cat8(trrd(p + (64 - sw)), trrd(p + (64 - sw) + 8 * 128));
    }
    __builtin_amdgcn_sched_barrier(0);
    wait_flags(ws, WS_F3 + ((rg >> 1) * 16 + kq * 4 + lane) * 128, lane < 4, tag);
    const unsigned ob = WS_OF + ((rg * 2 + rt) * 16 + kq * 4) * 2048;
#pragma unroll
    for (int i = 0; i < 8; ++i) og[i] = ldg(rs, l16 + (i & 3) * 1024, ob + (i >> 2) * 4096);
    __builtin_amdgcn_sched_barrier(0);
    lds_cptr wpa = (lds_cptr)lds + L_WOA + kq * 4 * 28 * 128 + wlo;
    f32x16 a0 = zero, a1 = zero;
#pragma unroll
    for (int i = 0; i < 8; ++i) {
      lds_cptr p = wpa + ((i >> 1) * 28 + 16 * (i & 1)) * 128;
      const bf16x8 wf0 = cat8(trrd(p + sw), trrd(p + sw + 8 * 128));
      const bf16x8 wf1 = cat8(trrd(p + (64 - sw)), trrd(p + (64 - sw) + 8 * 128));
      a0 = MFMA(wf0, of[i], a0);
      a1 = MFMA(wf1, of[i], a1);
    }
#pragma unroll
    for (int r = 0; r < 16; ++r) { a0[r] *= g; a1[r] *= g; }
    BARRIER();
    f32x16 b0 = zero, b1 = zero;
#pragma unroll
    for (int i = 0; i < 8; ++i) {
      b0 = MFMA(wb[2 * i], og[i], b0);
      b1 = MFMA(wb[2 * i + 1], og[i], b1);
    }
    const float g1 = 1.f - g;
#pragma unroll
    for (int r = 0; r < 16; ++r) { a0[r] += g1 * b0[r]; a1[r] += g1 * b1[r]; }
    lds_ptr ab = (lds_ptr)lds + L_WOA + rt * 32768 + kq * 2048 + l16;
#define WRQ(dst, A, o8)                                                                 \
  {                                                                                     \
    lds_ptr p = ab + (dst) * 8192;                                                      \
    *(lf4)p = (f32x4){A[o8], A[o8 + 1], A[o8 + 2], A[o8 + 3]};                          \
    *(lf4)(p + 1024) = (f32x4){A[o8 + 4], A[o8 + 5], A[o8 + 6], A[o8 + 7]};             \
  }
    WRQ(0, a0, 0) WRQ(1, a0, 8) WRQ(2, a1, 0) WRQ(3, a1, 8)
#undef WRQ
    BARRIER();
    {
      lds_ptr p = (lds_ptr)lds + L_WOA + rt * 32768 + kq * 8192 + l16;
      f32x4 u = *(lf4)p, w = *(lf4)(p + 1024);
#pragma unroll
      for (int src = 1; src < 4; ++src) { u += *(lf4)(p + src * 2048); w += *(lf4)(p + src * 2048 + 1024); }
      const int row = rg * 64 + rt * 32 + r32;
      const int col = cg3 * 64 + (kq >> 1) * 32 + (kq & 1) * 16 + 4 * hh;
      *(u32x2*)(out + (size_t)row * 448 + col) = (u32x2){pk2(u[0], u[1]), pk2(u[2], u[3])};
      *(u32x2*)(out + (size_t)row * 448 + col + 8) = (u32x2){pk2(w[0], w[1]), pk2(w[2], w[3])};
    }
  }
}

extern "C" void kernel_launch(void* const* d_in, const int* in_sizes, int n_in, void* d_out, int out_size,
                              void* d_ws, size_t ws_size, hipStream_t stream) {
  const u16* x = (const u16*)d_in[0];
  const u16* wqkvA = (const u16*)d_in[1];
  const u16* woA = (const u16*)d_in[2];
  const u16* wqkvB = (const u16*)d_in[4];
  const u16* woB = (const u16*)d_in[5];
  const u16* gw = (const u16*)d_in[6];
  (void)hipFuncSetAttribute((const void*)fused_k, hipFuncAttributeMaxDynamicSharedMemorySize, LDS_BYTES);
  hipLaunchKernelGGL(fused_k, dim3(256), dim3(512), LDS_BYTES, stream, x, wqkvA, wqkvB, woA, woB, gw, (char*)d_ws, (u16*)d_out);
}
```

```cpp
#include <hip/hip_runtime.h>
#include <hip/hip_bf16.h>
#include <stdint.h>

typedef unsigned short u16;
typedef unsigned long long u64;
typedef short bf16x8 __attribute__((ext_vector_type(8)));
typedef short s16x4 __attribute__((ext_vector_type(4)));
typedef float f32x16 __attribute__((ext_vector_type(16)));
typedef float f32x2 __attribute__((ext_vector_type(2)));
typedef float f32x4 __attribute__((ext_vector_type(4)));
typedef unsigned u32x4 __attribute__((ext_vector_type(4)));
typedef unsigned u32x2 __attribute__((ext_vector_type(2)));
typedef __bf16 bf2_t __attribute__((ext_vector_type(2)));
typedef __attribute__((address_space(3))) const char* lds_cptr;
typedef __attribute__((address_space(3))) char* lds_ptr;
typedef __attribute__((address_space(1))) u64 gu64;
typedef short v4i16_t __attribute__((ext_vector_type(4)));

#define DI __device__ __forceinline__
#define MFMA(a, b, c) __builtin_amdgcn_mfma_f32_32x32x16_bf16((a), (b), (c), 0, 0, 0)
#define BARRIER() asm volatile("s_waitcnt lgkmcnt(0)\n\ts_barrier" ::: "memory")
#define WAITBAR(N) asm volatile("s_waitcnt vmcnt(" #N ") lgkmcnt(0)\n\ts_barrier" ::: "memory")

extern "C" __device__ u64 gm_dispatch_id() __asm("llvm.amdgcn.dispatch.id");

#define CODE_LINES 288
constexpr float QSCALE = 0.18898223650461362f * 1.4426950408889634f;

constexpr unsigned WS_QF = 0;
constexpr unsigned WS_KV = 4u << 20;
constexpr unsigned WS_OF = 12u << 20;
constexpr unsigned WS_G = 16u << 20;
constexpr unsigned WS_FA = WS_G + 65536;
constexpr unsigned WS_FB = WS_FA + 32768;
constexpr unsigned WS_F3 = WS_FB + 32768;
constexpr unsigned WS_F3A = WS_F3 + 32768;
constexpr unsigned WS_END = WS_F3A + 131072;

constexpr int LDS_BYTES = 163840;
constexpr int L_X = 0;
constexpr int L_WR = 98304;
constexpr int L_QX = 153600;
constexpr int L_CNT = 163328;
constexpr int L_CB = 0;
constexpr int L_WOA = 65536;
constexpr int L_WOB1 = 123392;

DI unsigned pk2(float a, float b) {
  f32x2 v = {a, b};
  bf2_t r = __builtin_convertvector(v, bf2_t);
  return __builtin_bit_cast(unsigned, r);
}
DI u32x4 pack8u(const f32x16& x, int o) {
  u32x4 p;
  p[0] = pk2(x[o + 0], x[o + 1]); p[1] = pk2(x[o + 2], x[o + 3]);
  p[2] = pk2(x[o + 4], x[o + 5]); p[3] = pk2(x[o + 6], x[o + 7]);
  return p;
}
DI bf16x8 pack8(const f32x16& x, int o) { return __builtin_bit_cast(bf16x8, pack8u(x, o)); }
DI void glds16(const void* sbase, unsigned voff, unsigned lds_base) {
  unsigned sv;
  asm volatile("s_mov_b32 %0, m0\n\ts_mov_b32 m0, %3\n\ts_nop 4\n\tglobal_load_lds_dwordx4 %1, %2\n\ts_mov_b32 m0, %0"
               : "=&s"(sv) : "v"(voff), "s"(sbase), "s"(lds_base) : "memory");
}
DI s16x4 trrd(lds_cptr p) {
  return __builtin_bit_cast(s16x4, __builtin_amdgcn_ds_read_tr16_b64_v4i16((__attribute__((address_space(3))) v4i16_t*)p));
}
DI bf16x8 cat8(s16x4 lo, s16x4 hi) { return __builtin_shufflevector(lo, hi, 0, 1, 2, 3, 4, 5, 6, 7); }
DI bf16x8 ldsrd16(lds_cptr p) { return *(const __attribute__((address_space(3))) bf16x8*)p; }
DI float xhalf_max(float m) {
  auto rr = __builtin_amdgcn_permlane32_swap(__float_as_uint(m), __float_as_uint(m), false, false);
  return fmaxf(__uint_as_float(rr[0]), __uint_as_float(rr[1]));
}
DI float xhalf_sum(float m) {
  auto rr = __builtin_amdgcn_permlane32_swap(__float_as_uint(m), __float_as_uint(m), false, false);
  return __uint_as_float(rr[0]) + __uint_as_float(rr[1]);
}
DI float max16(const f32x16& s) {
  float a = fmaxf(fmaxf(s[0], s[1]), s[2]), b = fmaxf(fmaxf(s[3], s[4]), s[5]);
  a = fmaxf(fmaxf(a, s[6]), s[7]); b = fmaxf(fmaxf(b, s[8]), s[9]);
  a = fmaxf(fmaxf(a, s[10]), s[11]); b = fmaxf(fmaxf(b, s[12]), s[13]);
  a = fmaxf(fmaxf(a, s[14]), s[15]);
  return fmaxf(a, b);
}
DI bf16x8 ldg(__amdgpu_buffer_rsrc_t rs, unsigned voff, unsigned soff) {
  return __builtin_bit_cast(bf16x8, __builtin_amdgcn_raw_buffer_load_b128(rs, voff, soff, 16));
}
DI void stg(__amdgpu_buffer_rsrc_t rs, u32x4 v, unsigned voff, unsigned soff) {
  __builtin_amdgcn_raw_buffer_store_b128(v, rs, voff, soff, 16);
}
DI void wait_flags(const char* ws, unsigned off, bool active, u64 tag) {
  gu64* p = (gu64*)(ws + off);
  const u64 t0 = __builtin_amdgcn_s_memrealtime();
  for (;;) {
    u64 v = tag;
    if (active) v = __hip_atomic_load(p, __ATOMIC_RELAXED, __HIP_MEMORY_SCOPE_AGENT);
    if (__all(v == tag)) break;
    __builtin_amdgcn_s_sleep(1);
    if (__builtin_amdgcn_s_memrealtime() - t0 > 5000000ull) break;
  }
  __builtin_amdgcn_fence(__ATOMIC_ACQUIRE, "wavefront");
}
DI void set_flag(char* ws, unsigned off, u64 tag) {
  __hip_atomic_store((gu64*)(ws + off), tag, __ATOMIC_RELAXED, __HIP_MEMORY_SCOPE_AGENT);
}

DI void poll_fa(const char* ws, int lane, int wid, int h, int rb, u64 tag) {
  const int cw = (lane & 1) ? (28 * h + 27) >> 6 : (28 * h) >> 6;
  const int kind = lane >> 1;
  const int g = kind == 5 ? cw >> 1 : (kind == 2 || kind == 3) ? 7 + cw : (7 + cw) >> 1;
  const int r = kind == 5 ? rb : kind == 4 ? 0 : 2 * wid + (kind & 1);
  wait_flags(ws, WS_FA + (g * 16 + r) * 128, lane < (wid < 4 ? 12 : 8), tag);
}

typedef __attribute__((address_space(3))) unsigned* lds_u32p;
DI unsigned lds_arrive(char* lds, int idx) {
  return __hip_atomic_fetch_add((lds_u32p)((lds_ptr)lds + L_CNT) + idx, 1u, __ATOMIC_RELAXED, __HIP_MEMORY_SCOPE_WORKGROUP);
}
DI void lds_wait(char* lds, int idx, unsigned n) {
  while (__hip_atomic_load((lds_u32p)((lds_ptr)lds + L_CNT) + idx, __ATOMIC_RELAXED, __HIP_MEMORY_SCOPE_WORKGROUP) < n) __builtin_amdgcn_s_sleep(1);
  asm volatile("" ::: "memory");
}

struct P1Ctx {
  const char* x; const char* w1; const char* w2; const char* w3;
  unsigned lds0, xoff0, xoff1, woff;
  int wid, lane, rot;
};
DI int rotc(const P1Ctx& c, int ch) { const int t = ch + c.rot; return t >= 7 ? t - 7 : t; }
DI void p1_x(const P1Ctx& c, int ch) {
  const int slot = ch == 6 ? 0 : ch;
  const char* src = c.x + rotc(c, ch) * 128;
  glds16(src, c.xoff0, c.lds0 + L_X + slot * 16384 + c.wid * 1024);
  glds16(src, c.xoff1, c.lds0 + L_X + slot * 16384 + (c.wid + 8) * 1024);
}
DI void p1_w12(const P1Ctx& c, int ch) {
  const int slot = ch % 3;
  const int ro = rotc(c, ch) * (64 * 2688);
  glds16(c.w1 + ro, c.woff, c.lds0 + L_WR + slot * 16384 + c.wid * 1024);
  glds16(c.w2 + ro, c.woff, c.lds0 + L_WR + slot * 16384 + 8192 + c.wid * 1024);
}
DI constexpr int w3_off(int ch) { return L_X + (1 + (ch >> 1)) * 16384 + (ch & 1) * 8192; }
DI void p1_w3(const P1Ctx& c, int ch) {
  glds16(c.w3 + rotc(c, ch) * (64 * 2688), c.woff, c.lds0 + w3_off(ch) + c.wid * 1024);
}

template <bool ISV>
DI void p1_store(const f32x16& a, int br, int w, int ct, int t64, int lane, __amdgpu_buffer_rsrc_t rs) {
  const int r32 = lane & 31, hh = lane >> 5;
  const int sec = w / 7, wc = (w - sec * 7) * 64;
  if (ISV) {
    const int c = wc + ct * 32 + r32, head = c / 28, d = c - head * 28;
    const unsigned off = WS_KV + ((br * 16 + head) * 64 + t64) * 4096 + 2048 + (hh * 32 + d) * 16;
    stg(rs, pack8u(a, 0), off, 0);
    stg(rs, pack8u(a, 8), off + 1024, 0);
    if (d >= 24) {
      const unsigned f = d == 24 ? 0x3F803F80u : 0u;
      const u32x4 pv = {f, f, f, f};
      stg(rs, pv, off + 64, 0);
      stg(rs, pv, off + 64 + 1024, 0);
    }
  } else {
    const float sc = sec == 0 ? QSCALE : 1.f;
    unsigned w8[8];
#pragma unroll
    for (int i = 0; i < 8; ++i) w8[i] = pk2(a[2 * i] * sc, a[2 * i + 1] * sc);
    const unsigned padv = sec == 0 ? 0u : 0x3F80u;
    const unsigned hstride = sec == 0 ? 64u * 2048u : 64u * 4096u;
    const unsigned base0 = (sec == 0 ? WS_QF + (br * 16 * 64 + t64) * 2048 : WS_KV + (br * 16 * 64 + t64) * 4096) + r32 * 16;
#pragma unroll
    for (int pp = 0; pp < 2; ++pp) {
      const auto s0 = __builtin_amdgcn_permlane32_swap(w8[4 * pp], w8[4 * pp + 2], false, false);
      const auto s1 = __builtin_amdgcn_permlane32_swap(w8[4 * pp + 1], w8[4 * pp + 3], false, false);
      const int C = (wc >> 3) + ct * 4 + 2 * pp + hh, hp = C / 7, r = C - hp * 7;
      const int head = 2 * hp + (r > 3 ? 1 : 0), cc = r > 3 ? r - 3 : r;
      u32x4 v = {s0[0], s1[0], s0[1], s1[1]};
      if (r == 3) {
        const u32x4 v2 = {padv, 0u, s0[1], s1[1]};
        stg(rs, v2, base0 + (head + 1) * hstride, 0);
        v[2] = padv; v[3] = 0u;
      }
      stg(rs, v, base0 + head * hstride + (cc >> 1) * 1024 + (cc & 1) * 512, 0);
    }
  }
}

template <bool V1, bool V2>
DI void p1_sweep1(const P1Ctx& c, char* lds, bf16x8 (&xf)[28], f32x16& acc0, f32x16& acc1, int wl) {
  const int lane = c.lane, r32 = lane & 31, hh = lane >> 5;
  const int xrow = (c.wid & 3) * 32 + r32, xsw = (xrow >> 1) & 7;
  lds_cptr xb = (lds_cptr)lds + L_X + xrow * 128;
  bf16x8 wf[2][8];
#pragma unroll
  for (int ch = 0; ch < 8; ++ch) {
    if (ch == 7) __builtin_amdgcn_sched_barrier(0);
    else if (ch == 0) WAITBAR(8);
    else if (ch <= 4) WAITBAR(6);
    else if (ch == 5) WAITBAR(4);
    else if (ch == 6) WAITBAR(4);
    lds_cptr wb = (lds_cptr)lds + L_WR + (ch % 3) * 16384 + wl;
    lds_cptr xc = xb + (ch == 6 ? 0 : ch) * 16384;
#pragma unroll
    for (int s = 0; s < 4; ++s) {
      if (ch < 7) {
        xf[ch * 4 + s] = ldsrd16(xc + (((2 * s + hh) ^ xsw) * 16));
        lds_cptr wp = wb + s * 2048;
        wf[ch & 1][s] = cat8(trrd(wp), trrd(wp + 512));
        wf[ch & 1][4 + s] = cat8(trrd(wp + 8192), trrd(wp + 8192 + 512));
      }
      if (ch >= 1) {
        const bf16x8 x8 = xf[(ch - 1) * 4 + s];
        const bf16x8 w0 = wf[(ch - 1) & 1][s], w1 = wf[(ch - 1) & 1][4 + s];
        acc0 = V1 ? MFMA(x8, w0, acc0) : MFMA(w0, x8, acc0);
        acc1 = V2 ? MFMA(x8, w1, acc1) : MFMA(w1, x8, acc1);
      }
      __builtin_amdgcn_sched_barrier(0);
      if (s == 0 && ch >= 1 && ch <= 4) p1_w12(c, ch + 2);
      if (s == 1 && ch <= 3) p1_x(c, ch + 3);
      if (s == 0 && ch == 5) { p1_w3(c, 0); p1_w3(c, 1); }
      if (s == 1 && ch == 5) { p1_w3(c, 2); p1_w3(c, 3); }
      if (s == 0 && ch == 6) { p1_w3(c, 4); p1_w3(c, 5); }
      if (s == 1 && ch == 6) { p1_w3(c, 6); }
      __builtin_amdgcn_sched_barrier(0);
    }
  }
}
template <bool V3, int C0, int C1>
DI void p1_sweep2(char* lds, const bf16x8 (&xf)[28], f32x16& acc0, int wl) {
#pragma unroll
  for (int ch = C0; ch < C1; ++ch) {
    lds_cptr wb = (lds_cptr)lds + w3_off(ch) + wl;
#pragma unroll
    for (int s = 0; s < 4; ++s) {
      lds_cptr wp = wb + s * 2048;
      const bf16x8 w0 = cat8(trrd(wp), trrd(wp + 512));
      acc0 = V3 ? MFMA(xf[ch * 4 + s], w0, acc0) : MFMA(w0, xf[ch * 4 + s], acc0);
    }
  }
}

__global__ __launch_bounds__(512, 1) void fused_k(const u16* __restrict__ x, const u16* __restrict__ wA,
                                                  const u16* __restrict__ wB, const u16* __restrict__ woA,
                                                  const u16* __restrict__ woB, const u16* __restrict__ gw,
                                                  char* ws, u16* __restrict__ out) {
  extern __shared__ __attribute__((aligned(16))) char lds[];
  unsigned code_touch = 0;
  {
    u64 pc;
    asm volatile("s_getpc_b64 %0" : "=s"(pc));
    if (blockIdx.x < 40 && threadIdx.x >= 448)
      code_touch = *(const __attribute__((address_space(1))) unsigned*)((pc & ~127ull) + (u64)min((blockIdx.x >> 3) * 64 + (threadIdx.x & 63), CODE_LINES - 1) * 128);
  }
  asm volatile("" :: "s"(x), "s"(wA), "s"(wB), "s"(woA), "s"(woB), "s"(gw), "s"(ws), "s"(out));
  const int tid = threadIdx.x, lane = tid & 63;
  const int wid = __builtin_amdgcn_readfirstlane(tid >> 6);
  const int b = blockIdx.x;
  const int r32 = lane & 31, hh = lane >> 5;
  const int g16 = lane >> 4, i16 = lane & 15, q4 = i16 >> 2, p4 = i16 & 3, cb = g16 & 1;
  const unsigned lds0 = (unsigned)(uintptr_t)lds;
  const __amdgpu_buffer_rsrc_t rs = __builtin_amdgcn_make_buffer_rsrc((void*)ws, 0, (int)WS_END, 0x00020000);
  const u64 tag = ((gm_dispatch_id() + 1) * 0x9E3779B97F4A7C15ull) ^ (u64)(uintptr_t)__builtin_amdgcn_dispatch_ptr() ^ 0x5A17C3E9D2B40F61ull;
  const f32x16 zero = {};
  const unsigned l16 = lane * 16;
  const int h = (b & 7) + 8 * ((b >> 3) & 1), rb = b >> 4;
  unsigned fb_off = 0;
  f32x16 dA0 = zero, dA1 = zero;
  int d_w3 = 0, d_w2 = 0, d_n = 0, d_t64 = 0;
  if (tid < 8) ((__attribute__((address_space(3))) unsigned*)((lds_ptr)lds + L_CNT))[tid] = 0u;

  if (__builtin_expect(b < 224, 1)) {
    const int i5 = b >> 3, g = (b & 1) + 2 * (i5 % 7), rb1 = 4 * ((b & 7) >> 1) + i5 / 7;
    const int w1 = g < 7 ? 2 * g : 7 + g, w2 = g < 7 ? 2 * g + 1 : 2 * g - 7, w3 = g < 7 ? g : 2 * g - 6;
    const int br2 = g < 7 ? 0 : 1;
    P1Ctx c;
    c.x = (const char*)x; c.lds0 = lds0; c.wid = wid; c.lane = lane;
    c.rot = (i5 % 7 + 2 * (i5 / 7)) % 7;
    c.w1 = (const char*)wB + w1 * 128;
    c.w2 = (const char*)(g < 7 ? wB : wA) + w2 * 128;
    c.w3 = (const char*)wA + w3 * 128;
    {
      const int rl8 = lane >> 3, row0 = 8 * wid + rl8, row1 = row0 + 64;
      c.xoff0 = (unsigned)((rb1 * 128 + row0) * 896 + (((lane & 7) ^ ((row0 >> 1) & 7)) * 16));
      c.xoff1 = (unsigned)((rb1 * 128 + row1) * 896 + (((lane & 7) ^ ((row1 >> 1) & 7)) * 16));
      c.woff = (unsigned)(row0 * 2688 + (((lane & 7) ^ (((rl8 >> 1) & 1) << 2)) * 16));
    }
    p1_x(c, 0); p1_w12(c, 0);
    asm volatile("s_barrier" ::: "memory");
    p1_x(c, 1); p1_w12(c, 1); p1_x(c, 2); p1_w12(c, 2);
    const int rt = wid & 3, ct = wid >> 2, t64 = rb1 * 4 + rt;
    const int wl = (8 * hh + q4) * 128 + ((ct ^ (q4 >> 1)) * 64 + cb * 32 + p4 * 8);
    bf16x8 xf[28];
    f32x16 acc0 = zero, acc1 = zero;
    if (g < 7) p1_sweep1<false, false>(c, lds, xf, acc0, acc1, wl);
    else if (g < 11) p1_sweep1<true, false>(c, lds, xf, acc0, acc1, wl);
    else p1_sweep1<true, true>(c, lds, xf, acc0, acc1, wl);
    if (g < 7) {
      f32x16 cur = acc0;
#pragma unroll 1
      for (int it = 0; it < 2; ++it) { p1_store<false>(cur, 0, w1 + it, ct, t64, lane, rs); cur = acc1; }
    } else {
      p1_store<true>(acc0, 0, w1, ct, t64, lane, rs);
    }
    WAITBAR(4);
    acc0 = zero;
    if (g < 10) p1_sweep2<false, 0, 2>(lds, xf, acc0, wl); else p1_sweep2<true, 0, 2>(lds, xf, acc0, wl);
    asm volatile("s_waitcnt vmcnt(0)" ::: "memory");
    if (lane == 0) {
      const unsigned old = __hip_atomic_fetch_add((__attribute__((address_space(3))) unsigned*)((lds_ptr)lds + L_CNT + 4), 1u, __ATOMIC_RELAXED, __HIP_MEMORY_SCOPE_WORKGROUP);
      if (old == 7) { set_flag(ws, WS_FA + (g * 16 + rb1) * 128, tag); }
    }
    if (g < 10) p1_sweep2<false, 2, 7>(lds, xf, acc0, wl); else p1_sweep2<true, 2, 7>(lds, xf, acc0, wl);
    dA0 = acc0; dA1 = acc1; d_w3 = w3; d_w2 = w2; d_n = 1 + br2; d_t64 = t64;
    fb_off = WS_FB + (g * 16 + rb1) * 128;
  } else {
    typedef const __attribute__((address_space(1))) unsigned* gptr;
    const int q = (b - 224) >> 3;
    const int l0 = q * 784 + tid, l1 = q * 784 + min(tid + 512, 783);
    const unsigned t1 = *(gptr)((u64)woB + (u64)l0 * 128), t2 = *(gptr)((u64)woB + (u64)l1 * 128);
    const unsigned t3 = *(gptr)((u64)woA + (u64)l0 * 128), t4 = *(gptr)((u64)woA + (u64)l1 * 128);
    const unsigned acc = t1 ^ t2 ^ t3 ^ t4;
    asm volatile("" :: "v"(acc));
    {
      const int row0 = (b - 224) * 64 + wid * 8;
      const int lc = min(lane, 55);
      const u32x4 gv = *(const __attribute__((address_space(1))) u32x4*)((u64)gw + lc * 16);
      u32x4 xv[8];
#pragma unroll
      for (int i = 0; i < 8; ++i) xv[i] = *(const __attribute__((address_space(1))) u32x4*)((u64)x + (u64)(row0 + i) * 896 + lc * 16);
      float res = 0.f;
#pragma unroll
      for (int i = 0; i < 8; ++i) {
        float z = 0.f;
#pragma unroll
        for (int e = 0; e < 4; ++e) {
          const unsigned xe = xv[i][e], ge = gv[e];
          z = __builtin_amdgcn_fdot2_f32_bf16(__builtin_bit_cast(bf2_t, xe), __builtin_bit_cast(bf2_t, ge), z, false);
        }
        if (lane >= 56) z = 0.f;
#pragma unroll
        for (int o = 32; o >= 1; o >>= 1) z += __shfl_xor(z, o);
        if (lane == i) res = 1.0f / (1.0f + __expf(-z));
      }
      if (lane < 8) __builtin_amdgcn_raw_buffer_store_b32(__float_as_uint(res), rs, lane * 4, WS_G + row0 * 4, 16);
      WAITBAR(0);
      if (tid == 0) set_flag(ws, WS_FB + (14 * 16 + b - 224) * 128, tag);
    }
  }

  asm volatile("" :: "v"(code_touch));
  BARRIER();
  poll_fa(ws, lane, wid, h, rb, tag);
  typedef __attribute__((address_space(3))) f32x4* lf4;
  const int i5 = b >> 3, rg = (b & 7) + 8 * (i5 / 7), cg3 = i5 % 7;
  const unsigned wo_vo = (unsigned)(((wid & 3) * 8 + (lane >> 3)) * 896 + cg3 * 128 + (((lane & 7) ^ (((lane >> 4) & 1) << 2)) * 16));
#define WRT(dst, O)                                                                     \
  {                                                                                     \
    lds_ptr p_ = (lds_ptr)lds + L_CB + (dst) * 16384 + (wid & 3) * 4096 + l16;          \
    *(lf4)p_ = (f32x4){O[0], O[1], O[2], O[3]};                                         \
    *(lf4)(p_ + 1024) = (f32x4){O[4], O[5], O[6], O[7]};                                \
    *(lf4)(p_ + 2048) = (f32x4){O[8], O[9], O[10], O[11]};                              \
    *(lf4)(p_ + 3072) = (f32x4){O[12], O[13], O[14], O[15]};                            \
  }
  f32x16 o0 = zero, o1 = zero, o2 = zero, o3 = zero;
  {
    const unsigned kvh = WS_KV + (h * 64) * 4096;
    bf16x8 qw0, qw1, kr0, kr1;
    if (wid < 4) {
      const unsigned qb = WS_QF + (h * 64 + rb * 4 + wid) * 2048;
      qw0 = ldg(rs, l16, qb); qw1 = ldg(rs, l16 + 1024, qb);
      kr0 = ldg(rs, l16, kvh); kr1 = ldg(rs, l16 + 1024, kvh);
    }
    const unsigned kvw = kvh + wid * 8 * 4096;
#define TADDR(t) (kvw + (unsigned)((((t) + rb) & 7) * 4096))
    bf16x8 ka0 = ldg(rs, l16, TADDR(0)), ka1 = ldg(rs, l16 + 1024, TADDR(0));
    bf16x8 kb0 = ldg(rs, l16, TADDR(1)), kb1 = ldg(rs, l16 + 1024, TADDR(1));
    bf16x8 va0 = ldg(rs, l16 + 2048, TADDR(0)), va1 = ldg(rs, l16 + 3072, TADDR(0));
    bf16x8 vc0, vc1;
    if (wid < 4) {
      const bool odd = h & 1;
      f32x16 sr = MFMA(kr0, qw0, zero); sr = MFMA(kr1, qw1, sr);
      const unsigned nm = pk2(-xhalf_max(max16(sr)), 0.f);
      u32x4 ta = __builtin_bit_cast(u32x4, qw0), tb = __builtin_bit_cast(u32x4, qw1);
      if (odd ? !hh : hh) { if (odd) ta[0] = nm; else tb[2] = nm; }
      *(__attribute__((address_space(3))) u32x4*)((lds_ptr)lds + L_QX + wid * 2048 + l16) = ta;
      *(__attribute__((address_space(3))) u32x4*)((lds_ptr)lds + L_QX + wid * 2048 + 1024 + l16) = tb;
      asm volatile("s_waitcnt lgkmcnt(0)" ::: "memory");
      if (lane == 0) lds_arrive(lds, 5);
    }
    lds_wait(lds, 5, 4);
    bf16x8 kc0 = ldg(rs, l16, TADDR(2)), kc1 = ldg(rs, l16 + 1024, TADDR(2));
    bf16x8 vb0 = ldg(rs, l16 + 2048, TADDR(1)), vb1 = ldg(rs, l16 + 3072, TADDR(1));
    lds_cptr qx = (lds_cptr)lds + L_QX + l16;
    const bf16x8 q00 = ldsrd16(qx), q01 = ldsrd16(qx + 1024), q10 = ldsrd16(qx + 2048), q11 = ldsrd16(qx + 3072);
    const bf16x8 q20 = ldsrd16(qx + 4096), q21 = ldsrd16(qx + 5120), q30 = ldsrd16(qx + 6144), q31 = ldsrd16(qx + 7168);
    if (wid >= 4) __builtin_amdgcn_s_setprio(1);
    f32x16 s0 = MFMA(ka0, q00, zero); s0 = MFMA(ka1, q01, s0);
    f32x16 s1 = MFMA(ka0, q10, zero); s1 = MFMA(ka1, q11, s1);
    f32x16 s2 = MFMA(ka0, q20, zero); s2 = MFMA(ka1, q21, s2);
    f32x16 s3 = zero;
    u32x4 pa, pb0, pb1, pb2, pb3;
    {
      f32x16 cur = dA0;
      int wsel = d_w3;
#pragma unroll 1
      for (int it = 0; it < d_n; ++it) {
        if (wsel >= 14) p1_store<true>(cur, 1, wsel, wid >> 2, d_t64, lane, rs); else p1_store<false>(cur, 1, wsel, wid >> 2, d_t64, lane, rs);
        cur = dA1; wsel = d_w2;
      }
    }
    auto wo_prefetch = [&]() {
      const int hi4 = wid >> 2;
#pragma unroll
      for (int i = 0; i < 7; ++i) glds16((const char*)woA + (2 * i + hi4) * 32 * 896, wo_vo, lds0 + L_WOA + ((2 * i + hi4) * 4 + (wid & 3)) * 1024);
#pragma unroll
      for (int i = 0; i < 4; ++i)
        if (2 * i + hi4 < 7 || wid == 4) glds16((const char*)woB + (2 * i + hi4) * 32 * 896, wo_vo, lds0 + L_WOB1 + ((2 * i + hi4) * 4 + (wid & 3)) * 1024);
      if (wid == 5 && lane < 32) *(__attribute__((address_space(3))) u32x4*)((lds_ptr)lds + L_WOA + 448 * 128 + lane * 16) = (u32x4){0u, 0u, 0u, 0u};
    };
#define SB() __builtin_amdgcn_sched_barrier(0)
#define PIN(x) asm volatile("" : "+v"(x))
#define EX4(S, i) { S[i] = __builtin_amdgcn_exp2f(S[i]); S[i + 1] = __builtin_amdgcn_exp2f(S[i + 1]); S[i + 2] = __builtin_amdgcn_exp2f(S[i + 2]); S[i + 3] = __builtin_amdgcn_exp2f(S[i + 3]); }
#define BLK(S, O, PB, V0, PS, PO, PPB, PV1, PK0, PK1, PQA, PQB, DOPV, DOQK)              \
  {                                                                                      \
    if (DOPV) PO = MFMA(PV1, __builtin_bit_cast(bf16x8, PPB), PO);                       \
    EX4(S, 0) PIN(S); SB();                                                              \
    if (DOQK) PS = MFMA(PK0, PQA, zero);                                                 \
    EX4(S, 4) pa = pack8u(S, 0); PIN(S); PIN(pa); SB();                                  \
    if (DOQK) PS = MFMA(PK1, PQB, PS);                                                   \
    EX4(S, 8) PIN(S); SB();                                                              \
    O = MFMA(V0, __builtin_bit_cast(bf16x8, pa), O);                                     \
    EX4(S, 12) PB = pack8u(S, 8); PIN(PB); SB();                                         \
  }
#define STEP(t, V0, V1, K0, K1, PV0, PV1, PK0, PK1)                                      \
  {                                                                                      \
    BLK(s0, o0, pb0, V0, s3, o3, pb3, PV1, PK0, PK1, q30, q31, t > 0, true)              \
    if (t == 1 && fb_off) {   \
      asm volatile("s_waitcnt vmcnt(4)" ::: "memory");                                   \
      if (lane == 0) {                                                                   \
        const unsigned old = __hip_atomic_fetch_add((__attribute__((address_space(3))) unsigned*)((lds_ptr)lds + L_CNT), 1u, __ATOMIC_RELAXED, __HIP_MEMORY_SCOPE_WORKGROUP); \
        if (old == 7) set_flag(ws, fb_off, tag);                                         \
      }                                                                                  \
      SB();                                                                              \
    }                                                                                    \
    if (t + 3 < 8) { PK0 = ldg(rs, l16, TADDR(t + 3)); PK1 = ldg(rs, l16 + 1024, TADDR(t + 3)); }        \
    if (t + 2 < 8) { PV0 = ldg(rs, l16 + 2048, TADDR(t + 2)); PV1 = ldg(rs, l16 + 3072, TADDR(t + 2)); } \
    if (t == 1 && fb_off) wo_prefetch();                                                 \
    SB();                                                                                \
    if (t == 7 && wid < 4) { lds_wait(lds, 4, 4); SB(); }                                \
    BLK(s1, o1, pb1, V0, s0, o0, pb0, V1, K0, K1, q00, q01, true, t < 7)                 \
    if (t == 7) { WRT(0, o0) SB(); }                                                     \
    BLK(s2, o2, pb2, V0, s1, o1, pb1, V1, K0, K1, q10, q11, true, t < 7)                 \
    if (t == 7) { WRT(1, o1) SB(); }                                                     \
    BLK(s3, o3, pb3, V0, s2, o2, pb2, V1, K0, K1, q20, q21, true, t < 7)                 \
    if (t == 7) { WRT(2, o2) SB(); }                                                     \
  }
    STEP(0, va0, va1, kb0, kb1, vc0, vc1, ka0, ka1)
    STEP(1, vb0, vb1, kc0, kc1, va0, va1, kb0, kb1)
    STEP(2, vc0, vc1, ka0, ka1, vb0, vb1, kc0, kc1)
    STEP(3, va0, va1, kb0, kb1, vc0, vc1, ka0, ka1)
    STEP(4, vb0, vb1, kc0, kc1, va0, va1, kb0, kb1)
    STEP(5, vc0, vc1, ka0, ka1, vb0, vb1, kc0, kc1)
    STEP(6, va0, va1, kb0, kb1, vc0, vc1, ka0, ka1)
    STEP(7, vb0, vb1, kc0, kc1, va0, va1, kb0, kb1)
    o3 = MFMA(vb1, __builtin_bit_cast(bf16x8, pb3), o3);
    WRT(3, o3)
#undef TADDR
#undef BLK
#undef EX4
#undef STEP
  }
#define RDADD(ACC)                                                                      \
  {                                                                                     \
    lds_ptr p_ = (lds_ptr)lds + L_CB + (wid & 3) * 16384 + l16;                         \
    _Pragma("unroll") for (int src = 0; src < 4; ++src)                                 \
      _Pragma("unroll") for (int q_ = 0; q_ < 4; ++q_) {                                \
        const f32x4 t_ = *(lf4)(p_ + src * 4096 + q_ * 1024);                           \
        ACC[4 * q_] += t_[0]; ACC[4 * q_ + 1] += t_[1]; ACC[4 * q_ + 2] += t_[2]; ACC[4 * q_ + 3] += t_[3]; \
      }                                                                                 \
  }
  f32x16 oh = zero;
  if (wid >= 4) {
    asm volatile("s_waitcnt lgkmcnt(0)" ::: "memory");
    if (lane == 0) lds_arrive(lds, 3);
    {
      const int pr = rb > 0 ? rb - 1 : rb;
      const int cw = (lane & 1) ? (28 * h + 27) >> 6 : (28 * h) >> 6;
      const int kind = lane >> 1;
      const int gg = kind == 0 ? cw : (kind & 1) ? 7 + (cw >> 1) : 7 + ((7 + cw) >> 1);
      wait_flags(ws, WS_FB + (gg * 16 + (kind >= 3 ? pr : rb)) * 128, lane < 10, tag);
    }
    const int t64 = rb * 4 + wid - 4;
    const unsigned qo = WS_QF + ((16 + h) * 64 + t64) * 2048;
    const unsigned kc = WS_KV + ((16 + h) * 64 + t64) * 4096;
    const unsigned kp = t64 > 0 ? kc - 4096 : kc;
    const bf16x8 q0 = ldg(rs, l16, qo), q1 = ldg(rs, l16 + 1024, qo);
    const bf16x8 kc0 = ldg(rs, l16, kc), kc1 = ldg(rs, l16 + 1024, kc), vc0 = ldg(rs, l16 + 2048, kc), vc1 = ldg(rs, l16 + 3072, kc);
    const bf16x8 kp0 = ldg(rs, l16, kp), kp1 = ldg(rs, l16 + 1024, kp), vp1 = ldg(rs, l16 + 3072, kp);
    lds_wait(lds, 3, 4);
    RDADD(oh)
    asm volatile("s_waitcnt lgkmcnt(0)" ::: "memory");
    if (lane == 0) lds_arrive(lds, 4);
    f32x16 sc = MFMA(kc0, q0, zero); sc = MFMA(kc1, q1, sc);
    f32x16 sp = MFMA(kp0, q0, zero); sp = MFMA(kp1, q1, sp);
    const u64 mk = (0x5555ull << (r32 + 18)) >> (4 * hh);
    const int mcur = (int)(unsigned)(mk >> 32), mprev = t64 > 0 ? (int)(unsigned)mk : 0;
#pragma unroll
    for (int r = 0; r < 16; ++r) {
      const int i = (r & 3) + 8 * (r >> 2);
      const unsigned selc = (unsigned)__builtin_amdgcn_sbfe(mcur, i, 1);
      sc[r] = __uint_as_float((__float_as_uint(sc[r]) & selc) | (0xFF800000u & ~selc));
      if (r >= 8) {
        const unsigned selp = (unsigned)__builtin_amdgcn_sbfe(mprev, i, 1);
        sp[r] = __uint_as_float((__float_as_uint(sp[r]) & selp) | (0xFF800000u & ~selp));
      }
    }
    float mx = max16(sc);
#pragma unroll
    for (int r = 8; r < 16; r += 2) mx = fmaxf(fmaxf(mx, sp[r]), sp[r + 1]);
    const float m = xhalf_max(mx);
#pragma unroll
    for (int r = 0; r < 16; ++r) {
      sc[r] = __builtin_amdgcn_exp2f(sc[r] - m);
      if (r >= 8) sp[r] = __builtin_amdgcn_exp2f(sp[r] - m);
    }
    f32x16 o = MFMA(vc0, pack8(sc, 0), zero);
    o = MFMA(vc1, pack8(sc, 8), o);
    o = MFMA(vp1, pack8(sp, 8), o);
    const float l = __shfl(o[12], 32 + r32);
    const float rl = 1.0f / l;
#pragma unroll
    for (int r = 0; r < 16; ++r) o[r] *= rl;
    if (hh) { o[12] = 0.f; o[13] = 0.f; o[14] = 0.f; o[15] = 0.f; }
    const unsigned od = WS_OF + ((64 + t64) * 16 + h) * 2048;
    stg(rs, pack8u(o, 0), l16, od);
    stg(rs, pack8u(o, 8), l16 + 1024, od);
    asm volatile("s_waitcnt vmcnt(0)" ::: "memory");
    if (lane == 0) set_flag(ws, WS_F3A + (t64 * 16 + h) * 128, tag);
  }
  BARRIER();
  if (wid >= 4) {
    RDADD(oh)
    float l = __shfl(oh[12], 32 + r32);
    const int t64 = rb * 4 + wid - 4;
    if (__any(!(l < 1.0e18f && l > 1.0e-18f))) {
      const unsigned qo = WS_QF + (h * 64 + t64) * 2048;
      const bf16x8 q0 = ldg(rs, l16, qo), q1 = ldg(rs, l16 + 1024, qo);
      float m = -INFINITY;
      oh = zero;
#pragma unroll 1
      for (int t = 0; t < 64; ++t) {
        const unsigned kv = WS_KV + (h * 64 + t) * 4096;
        const bf16x8 k0 = ldg(rs, l16, kv), k1 = ldg(rs, l16 + 1024, kv), v0 = ldg(rs, l16 + 2048, kv), v1 = ldg(rs, l16 + 3072, kv);
        f32x16 sx = MFMA(k0, q0, zero); sx = MFMA(k1, q1, sx);
        const float mn = fmaxf(m, xhalf_max(max16(sx)));
        const float al = __builtin_amdgcn_exp2f(m - mn);
        m = mn;
#pragma unroll
        for (int r = 0; r < 16; ++r) { oh[r] *= al; sx[r] = __builtin_amdgcn_exp2f(sx[r] - mn); }
        oh = MFMA(v0, pack8(sx, 0), oh);
        oh = MFMA(v1, pack8(sx, 8), oh);
      }
      l = __shfl(oh[12], 32 + r32);
    }
    const float rl = 1.0f / l;
#pragma unroll
    for (int r = 0; r < 16; ++r) oh[r] *= rl;
    if (hh) { oh[12] = 0.f; oh[13] = 0.f; oh[14] = 0.f; oh[15] = 0.f; }
    const unsigned od = WS_OF + (t64 * 16 + h) * 2048;
    stg(rs, pack8u(oh, 0), l16, od);
    stg(rs, pack8u(oh, 8), l16 + 1024, od);
  }
#undef WRT
#undef RDADD
  BARRIER();
  if (wid >= 4) {
    if (b >= 224) {
      asm volatile("s_waitcnt vmcnt(0)" ::: "memory");
      if (lane == 0 && lds_arrive(lds, 2) == 3) set_flag(ws, WS_F3 + (rb * 16 + h) * 128, tag);
    }
  } else if (b < 224) {
#pragma unroll
    for (int i = 0; i < 7; ++i) glds16((const char*)woB + (224 + i * 32) * 896, wo_vo, lds0 + L_CB + (i * 4 + wid) * 1024);
    if (wid == 0 && lane < 32) *(__attribute__((address_space(3))) u32x4*)((lds_ptr)lds + L_CB + 224 * 128 + lane * 16) = (u32x4){0u, 0u, 0u, 0u};
  }
  if (b >= 224) return;

  {
    const int rt = wid & 1, kq = wid >> 1;
    wait_flags(ws, lane < 4 ? WS_F3A + ((rg * 2 + rt) * 16 + kq * 4 + lane) * 128 : WS_FB + (14 * 16 + rg) * 128,
               lane < 5, tag);
    if (wid >= 4) {
      asm volatile("s_waitcnt vmcnt(0)" ::: "memory");
      if (lane == 0 && lds_arrive(lds, 2) == 3) { set_flag(ws, WS_F3 + (rb * 16 + h) * 128, tag); }
    }
    const float g = __uint_as_float(__builtin_amdgcn_raw_buffer_load_b32(rs, r32 * 4, WS_G + (rg * 2 + rt) * 128, 16));
    const unsigned oa = WS_OF + ((64 + rg * 2 + rt) * 16 + kq * 4) * 2048;
    bf16x8 of[8], og[8];
#pragma unroll
    for (int i = 0; i < 8; ++i) of[i] = ldg(rs, l16 + (i & 3) * 1024, oa + (i >> 2) * 4096);
    WAITBAR(9);
    __builtin_amdgcn_s_setprio(0);
    const int wlo = (4 * hh + q4) * 128 + cb * 32 + p4 * 8;
    const int sw = (q4 >> 1) * 64;
    lds_cptr wpb = (lds_cptr)lds + (kq < 2 ? L_WOB1 + kq * 4 * 28 * 128 : L_CB + (kq - 2) * 4 * 28 * 128) + wlo;
    bf16x8 wb[16];
#pragma unroll
    for (int i = 0; i < 8; ++i) {
      lds_cptr p = wpb + ((i >> 1) * 28 + 16 * (i & 1)) * 128;
      wb[2 * i] = cat8(trrd(p + sw), trrd(p + sw + 8 * 128));
      wb[2 * i + 1] = cat8(trrd(p + (64 - sw)), trrd(p + (64 - sw) + 8 * 128));
    }
    __builtin_amdgcn_sched_barrier(0);
    wait_flags(ws, WS_F3 + ((rg >> 1) * 16 + kq * 4 + lane) * 128, lane < 4, tag);
    const unsigned ob = WS_OF + ((rg * 2 + rt) * 16 + kq * 4) * 2048;
#pragma unroll
    for (int i = 0; i < 8; ++i) og[i] = ldg(rs, l16 + (i & 3) * 1024, ob + (i >> 2) * 4096);
    __builtin_amdgcn_sched_barrier(0);
    lds_cptr wpa = (lds_cptr)lds + L_WOA + kq * 4 * 28 * 128 + wlo;
    f32x16 a0 = zero, a1 = zero;
#pragma unroll
    for (int i = 0; i < 8; ++i) {
      lds_cptr p = wpa + ((i >> 1) * 28 + 16 * (i & 1)) * 128;
      const bf16x8 wf0 = cat8(trrd(p + sw), trrd(p + sw + 8 * 128));
      const bf16x8 wf1 = cat8(trrd(p + (64 - sw)), trrd(p + (64 - sw) + 8 * 128));
      a0 = MFMA(wf0, of[i], a0);
      a1 = MFMA(wf1, of[i], a1);
    }
#pragma unroll
    for (int r = 0; r < 16; ++r) { a0[r] *= g; a1[r] *= g; }
    BARRIER();
    f32x16 b0 = zero, b1 = zero;
#pragma unroll
    for (int i = 0; i < 8; ++i) {
      b0 = MFMA(wb[2 * i], og[i], b0);
      b1 = MFMA(wb[2 * i + 1], og[i], b1);
    }
    const float g1 = 1.f - g;
#pragma unroll
    for (int r = 0; r < 16; ++r) { a0[r] += g1 * b0[r]; a1[r] += g1 * b1[r]; }
    lds_ptr ab = (lds_ptr)lds + L_WOA + rt * 32768 + kq * 2048 + l16;
#define WRQ(dst, A, o8)                                                                 \
  {                                                                                     \
    lds_ptr p = ab + (dst) * 8192;                                                      \
    *(lf4)p = (f32x4){A[o8], A[o8 + 1], A[o8 + 2], A[o8 + 3]};                          \
    *(lf4)(p + 1024) = (f32x4){A[o8 + 4], A[o8 + 5], A[o8 + 6], A[o8 + 7]};             \
  }
    WRQ(0, a0, 0) WRQ(1, a0, 8) WRQ(2, a1, 0) WRQ(3, a1, 8)
#undef WRQ
    BARRIER();
    {
      lds_ptr p = (lds_ptr)lds + L_WOA + rt * 32768 + kq * 8192 + l16;
      f32x4 u = *(lf4)p, w = *(lf4)(p + 1024);
#pragma unroll
      for (int src = 1; src < 4; ++src) { u += *(lf4)(p + src * 2048); w += *(lf4)(p + src * 2048 + 1024); }
      const int row = rg * 64 + rt * 32 + r32;
      const auto e0 = __builtin_amdgcn_permlane32_swap(pk2(u[0], u[1]), pk2(w[0], w[1]), false, false);
      const auto e1 = __builtin_amdgcn_permlane32_swap(pk2(u[2], u[3]), pk2(w[2], w[3]), false, false);
      const int col8 = cg3 * 64 + (kq >> 1) * 32 + (kq & 1) * 16 + 8 * hh;
      const __amdgpu_buffer_rsrc_t ro = __builtin_amdgcn_make_buffer_rsrc((void*)out, 0, 2048 * 448 * 2, 0x00020000);
      __builtin_amdgcn_raw_buffer_store_b128((u32x4){e0[0], e1[0], e0[1], e1[1]}, ro, (row * 448 + col8) * 2, 0, 0);
    }
  }
}

extern "C" void kernel_launch(void* const* d_in, const int* in_sizes, int n_in, void* d_out, int out_size,
                              void* d_ws, size_t ws_size, hipStream_t stream) {
  const u16* x = (const u16*)d_in[0];
  const u16* wqkvA = (const u16*)d_in[1];
  const u16* woA = (const u16*)d_in[2];
  const u16* wqkvB = (const u16*)d_in[4];
  const u16* woB = (const u16*)d_in[5];
  const u16* gw = (const u16*)d_in[6];
  (void)hipFuncSetAttribute((const void*)fused_k, hipFuncAttributeMaxDynamicSharedMemorySize, LDS_BYTES);
  hipLaunchKernelGGL(fused_k, dim3(256), dim3(512), LDS_BYTES, stream, x, wqkvA, wqkvB, woA, woB, gw, (char*)d_ws, (u16*)d_out);
}
```
